```python
import torch

def _softmax(x, axis=-1):
    return torch.softmax(x, dim=axis)

def _gelu(x):
    return torch.nn.functional.gelu(x, approximate='tanh')

def _silu(x):
    return torch.nn.functional.silu(x)

def _sigmoid(x):
    return torch.sigmoid(x)

def _layernorm(x, axis=-1, eps=1e-05):
    m = x.mean(dim=axis, keepdim=True)
    v = x.var(dim=axis, keepdim=True, unbiased=False)
    return (x - m) / torch.sqrt(v + eps)

def _scatter_add(tgt, idx, src, axis=0):
    return tgt.clone().index_add_(axis, idx, src)

def _segment_sum(src, seg, n):
    out = torch.zeros((n,) + src.shape[1:], dtype=src.dtype)
    return out.index_add_(0, seg, src)

def _pad(x, pw):
    flat = tuple((v for pair in reversed(pw) for v in pair))
    return torch.nn.functional.pad(x, flat)
kernel_name = 'adaLN_cond__mha_block_cached__glu_gated_glu__decode'

def setup_inputs():

    _w = lambda a: (lambda t: t.to(torch.bfloat16) if t.is_floating_point() else t)(torch.as_tensor(a))

    def _A_setup():
        B, Sq, D = (2, 1, 448)
        H, Sk, Dh = (16, 8192, 256)
        return {'x': _w((lambda *s: torch.randn(*s, dtype=torch.bfloat16) * 0.05)(B, Sq, D)), 'k_cache': _w((lambda *s: torch.randn(*s, dtype=torch.bfloat16) * 3.2)(B, H, Sk, Dh)), 'v_cache': _w((lambda *s: torch.randn(*s, dtype=torch.bfloat16) * 0.4)(B, H, Sk, Dh)), 'wq': _w((lambda *s: torch.randn(*s, dtype=torch.bfloat16) * 3.2)(D, H * Dh) * D ** (-0.5)), 'wo': _w((lambda *s: torch.randn(*s, dtype=torch.bfloat16) * 0.4)(H * Dh, D) * (H * Dh) ** (-0.5))}
    _A_in = _A_setup()

    def _B_setup():
        B, S, D, F = (2, 1, 448, 1792)
        return {'x': _w((lambda *s: torch.randn(*s, dtype=torch.bfloat16) * 0.05)(B, S, D)), 'w_gate': _w((lambda *s: torch.randn(*s, dtype=torch.bfloat16) * 0.05)(D, F)), 'w_up': _w((lambda *s: torch.randn(*s, dtype=torch.bfloat16) * 0.05)(D, F)), 'w_down': _w((lambda *s: torch.randn(*s, dtype=torch.bfloat16) * 0.05)(F, D))}
    _B_in = _B_setup()
    return {'A_x': _A_in['x'], 'A_k_cache': _A_in['k_cache'], 'A_v_cache': _A_in['v_cache'], 'A_wq': _A_in['wq'], 'A_wo': _A_in['wo'], 'B_x': _B_in['x'], 'B_w_gate': _B_in['w_gate'], 'B_w_up': _B_in['w_up'], 'B_w_down': _B_in['w_down'], 'c': _w((lambda *s: torch.randn(*s, dtype=torch.bfloat16) * 0.1)(2, 448) * 8.0), 'w_mod': _w((lambda *s: torch.randn(*s, dtype=torch.bfloat16) * 0.1)(448, 6 * 448) * 448 ** (-0.5) * 8.0)}

def reference(A_x, A_k_cache, A_v_cache, A_wq, A_wo, B_x, B_w_gate, B_w_up, B_w_down, c, w_mod):

    def _A_ref(x, k_cache, v_cache, wq, wo):
        B, Sq, D = x.shape
        H, Dh = (16, 256)
        q = (lambda x, *d: x.permute(*d))((x @ wq).reshape(B, Sq, H, Dh), 0, 2, 1, 3)
        scores = torch.einsum('bhqd,bhkd->bhqk', q, k_cache) * Dh ** (-0.5)
        p = _softmax(scores, axis=-1)
        o = torch.einsum('bhqk,bhkd->bhqd', p, v_cache)
        return (lambda x, *d: x.permute(*d))(o, 0, 2, 1, 3).reshape(B, Sq, H * Dh) @ wo

    def _B_ref(x, w_gate, w_up, w_down):
        g = torch.einsum('bsd,df->bsf', x, w_gate)
        u = torch.einsum('bsd,df->bsf', x, w_up)
        h = _sigmoid(g) * u
        return torch.einsum('bsf,fd->bsd', h, w_down)
    x = A_x
    mod = (_silu(c) @ w_mod).reshape(c.shape[0], 6, 1, -1)
    sa, ca, ga, sb, cb, gb = (mod[:, 0].clone(), mod[:, 1].clone(), mod[:, 2].clone(), mod[:, 3].clone(), mod[:, 4].clone(), mod[:, 5].clone())
    h = _layernorm(x, axis=-1) * (1 + ca) + sa
    x = x + ga * _A_ref(h, A_k_cache, A_v_cache, A_wq, A_wo)
    h = _layernorm(x, axis=-1) * (1 + cb) + sb
    x = x + gb * _B_ref(h, B_w_gate, B_w_up, B_w_down)
    return x
```

```cpp
#include <hip/hip_runtime.h>
#include <hip/hip_bf16.h>
#include <stdint.h>

#define ROUND_SCORES 1

typedef unsigned short bf16_t;
typedef unsigned u32x4 __attribute__((ext_vector_type(4)));
typedef __bf16 bf16x2 __attribute__((ext_vector_type(2)));

constexpr int NB = 2, DM = 448, NH = 16, SK = 8192, DH = 256, FF = 1792, NMOD = 6 * DM;
constexpr int NSPLIT = 16;

__device__ __forceinline__ float bf2f(bf16_t v) { return __uint_as_float(((unsigned)v) << 16); }
__device__ __forceinline__ float rbf(float f) {
  __bf16 b = (__bf16)f; return (float)b;
}
__device__ __forceinline__ bf16_t f2bf(float f) { __bf16 b = (__bf16)f; return __builtin_bit_cast(bf16_t, b); }
__device__ __forceinline__ float dot2(unsigned k, unsigned q, float acc) {
  return __builtin_amdgcn_fdot2_f32_bf16(__builtin_bit_cast(bf16x2, k), __builtin_bit_cast(bf16x2, q), acc, false);
}

__global__ void k_mod(const bf16_t* c, const bf16_t* w_mod, float* mod) {
  __shared__ float sc[NB][DM];
  __shared__ float red[4][NB][64];
  for (int i = threadIdx.x; i < NB * DM; i += 256) {
    float v = bf2f(c[i]);
    sc[i / DM][i % DM] = rbf(v / (1.f + __expf(-v)));
  }
  __syncthreads();
  int lane = threadIdx.x & 63, w = threadIdx.x >> 6;
  int j = blockIdx.x * 64 + lane;
  float a0 = 0, a1 = 0;
  for (int d = w; d < DM; d += 4) { float x = bf2f(w_mod[d * NMOD + j]); a0 += sc[0][d] * x; a1 += sc[1][d] * x; }
  red[w][0][lane] = a0; red[w][1][lane] = a1;
  __syncthreads();
  if (w < 2) mod[w * NMOD + j] = rbf(red[0][w][lane] + red[1][w][lane] + red[2][w][lane] + red[3][w][lane]);
}

__device__ void ln_mod(const float (*xin)[DM], float (*h)[DM], const float* mod, int si, int ci, float* tmp) {
  int lane = threadIdx.x & 63, w = threadIdx.x >> 6;
  if (w < NB) {
    float s = 0;
    for (int d = lane; d < DM; d += 64) s += xin[w][d];
    for (int o = 32; o; o >>= 1) s += __shfl_xor(s, o);
    float mf = s / DM;
    float v = 0;
    for (int d = lane; d < DM; d += 64) { float t = xin[w][d] - mf; v += t * t; }
    for (int o = 32; o; o >>= 1) v += __shfl_xor(v, o);
    float m = rbf(mf), var = rbf(v / DM);
    float den = rbf(sqrtf(rbf(var + 1e-5f)));
    for (int d = lane; d < DM; d += 64) {
      float n = rbf(rbf(xin[w][d] - m) / den);
      float t = rbf(n * rbf(1.f + mod[w * NMOD + ci * DM + d]));
      h[w][d] = rbf(t + mod[w * NMOD + si * DM + d]);
    }
  }
  __syncthreads();
}

__global__ void k_q(const bf16_t* x, const float* mod, const bf16_t* wq, bf16_t* q) {
  __shared__ float xs[NB][DM], h[NB][DM];
  __shared__ float red[4][NB][64];
  for (int i = threadIdx.x; i < NB * DM; i += 256) xs[i / DM][i % DM] = bf2f(x[i]);
  __syncthreads();
  ln_mod(xs, h, mod, 0, 1, nullptr);
  int lane = threadIdx.x & 63, w = threadIdx.x >> 6;
  int j = blockIdx.x * 64 + lane;
  float a0 = 0, a1 = 0;
  for (int d = w; d < DM; d += 4) { float xx = bf2f(wq[d * (NH * DH) + j]); a0 += h[0][d] * xx; a1 += h[1][d] * xx; }
  red[w][0][lane] = a0; red[w][1][lane] = a1;
  __syncthreads();
  if (w < 2) q[w * NH * DH + j] = f2bf(red[0][w][lane] + red[1][w][lane] + red[2][w][lane] + red[3][w][lane]);
}

__global__ void __launch_bounds__(256) k_attn(const bf16_t* q, const bf16_t* kc, const bf16_t* vc, float* pm, float* po) {
  __shared__ float mrg[4][2][260];
  int s = blockIdx.x, bh = blockIdx.y;
  int lane = threadIdx.x & 63, w = threadIdx.x >> 6, hf = lane >> 5, l32 = lane & 31;
  constexpr int KPS = SK / NSPLIT, KPW = KPS / 4;
  u32x4 qv = *(const u32x4*)(q + bh * DH + l32 * 8);
  size_t base = ((size_t)bh * SK + s * KPS + w * KPW) * DH + lane * 8;
  const u32x4* kp = (const u32x4*)(kc + base);
  const u32x4* vp = (const u32x4*)(vc + base);
  float m = -INFINITY, l = 0, acc[8] = {0, 0, 0, 0, 0, 0, 0, 0};
  for (int it = 0; it < KPW / 8; ++it) {
    u32x4 K[4], V[4];
#pragma unroll
    for (int t = 0; t < 4; ++t) { K[t] = __builtin_nontemporal_load(kp + (it * 4 + t) * 64); V[t] = __builtin_nontemporal_load(vp + (it * 4 + t) * 64); }
    float sc[4];
#pragma unroll
    for (int t = 0; t < 4; ++t) {
      float d = 0;
#pragma unroll
      for (int k = 0; k < 4; ++k) d = dot2(K[t][k], qv[k], d);
      for (int o = 16; o; o >>= 1) d += __shfl_xor(d, o);
      d = rbf(d);
      sc[t] = d * 0.0625f;
    }
    float mx = fmaxf(fmaxf(sc[0], sc[1]), fmaxf(sc[2], sc[3]));
    float mn = fmaxf(m, mx);
    float al = __expf(m - mn);
    m = mn;
    l *= al;
#pragma unroll
    for (int k = 0; k < 8; ++k) acc[k] *= al;
#pragma unroll
    for (int t = 0; t < 4; ++t) {
      float p = __expf(sc[t] - mn);
      l += p;
#pragma unroll
      for (int k = 0; k < 4; ++k) {
        acc[2 * k] += p * __uint_as_float(V[t][k] << 16);
        acc[2 * k + 1] += p * __uint_as_float(V[t][k] & 0xffff0000u);
      }
    }
  }
  for (int k = 0; k < 8; ++k) mrg[w][hf][l32 * 8 + k] = acc[k];
  if (l32 == 0) { mrg[w][hf][256] = m; mrg[w][hf][257] = l; }
  __syncthreads();
  float M = -INFINITY;
  for (int i = 0; i < 8; ++i) M = fmaxf(M, mrg[i >> 1][i & 1][256]);
  float L = 0, o = 0;
  for (int i = 0; i < 8; ++i) { float wt = __expf(mrg[i >> 1][i & 1][256] - M); L += wt * mrg[i >> 1][i & 1][257]; o += wt * mrg[i >> 1][i & 1][threadIdx.x]; }
  po[(bh * NSPLIT + s) * DH + threadIdx.x] = o;
  if (threadIdx.x == 0) { pm[(bh * NSPLIT + s) * 2] = M; pm[(bh * NSPLIT + s) * 2 + 1] = L; }
}

__global__ void k_comb(const float* pm, const float* po, float* o) {
  int bh = blockIdx.x;
  float M = -INFINITY;
  for (int s = 0; s < NSPLIT; ++s) M = fmaxf(M, pm[(bh * NSPLIT + s) * 2]);
  float L = 0, a = 0;
  for (int s = 0; s < NSPLIT; ++s) { float wt = __expf(pm[(bh * NSPLIT + s) * 2] - M); L += wt * pm[(bh * NSPLIT + s) * 2 + 1]; a += wt * po[(bh * NSPLIT + s) * DH + threadIdx.x]; }
  o[bh * DH + threadIdx.x] = rbf(a / L);
}

__global__ void k_wo(const float* o, const bf16_t* wo, const bf16_t* x, const float* mod, float* x1) {
  __shared__ float red[32][NB][8];
  int c = threadIdx.x & 7, g = threadIdx.x >> 3;
  int j = blockIdx.x * 8 + c;
  float a0 = 0, a1 = 0;
  for (int k = g; k < NH * DH; k += 32) { float w = bf2f(wo[k * DM + j]); a0 += o[k] * w; a1 += o[NH * DH + k] * w; }
  red[g][0][c] = a0; red[g][1][c] = a1;
  __syncthreads();
  if (threadIdx.x < 16) {
    int b = threadIdx.x >> 3; float s = 0;
    for (int i = 0; i < 32; ++i) s += red[i][b][c];
    float att = rbf(s);
    x1[b * DM + j] = rbf(bf2f(x[b * DM + j]) + rbf(mod[b * NMOD + 2 * DM + j] * att));
  }
}

__global__ void k_gu(const float* x1, const float* mod, const bf16_t* wg, const bf16_t* wu, float* hh) {
  __shared__ float xs[NB][DM], h[NB][DM];
  __shared__ float red[16][4][16];
  for (int i = threadIdx.x; i < NB * DM; i += 256) xs[i / DM][i % DM] = x1[i];
  __syncthreads();
  ln_mod(xs, h, mod, 3, 4, nullptr);
  int c = threadIdx.x & 15, g = threadIdx.x >> 4;
  int j = blockIdx.x * 16 + c;
  float g0 = 0, g1 = 0, u0 = 0, u1 = 0;
  for (int d = g; d < DM; d += 16) { float a = bf2f(wg[d * FF + j]), b = bf2f(wu[d * FF + j]); g0 += h[0][d] * a; g1 += h[1][d] * a; u0 += h[0][d] * b; u1 += h[1][d] * b; }
  red[g][0][c] = g0; red[g][1][c] = g1; red[g][2][c] = u0; red[g][3][c] = u1;
  __syncthreads();
  if (threadIdx.x < 32) {
    int b = threadIdx.x >> 4; float gs = 0, us = 0;
    for (int i = 0; i < 16; ++i) { gs += red[i][b][c]; us += red[i][2 + b][c]; }
    gs = rbf(gs); us = rbf(us);
    float sg = rbf(1.f / (1.f + __expf(-gs)));
    hh[b * FF + j] = rbf(sg * us);
  }
}

__global__ void k_down(const float* hh, const bf16_t* wd, const float* x1, const float* mod, bf16_t* out) {
  __shared__ float red[32][NB][8];
  int c = threadIdx.x & 7, g = threadIdx.x >> 3;
  int j = blockIdx.x * 8 + c;
  float a0 = 0, a1 = 0;
  for (int k = g; k < FF; k += 32) { float w = bf2f(wd[k * DM + j]); a0 += hh[k] * w; a1 += hh[FF + k] * w; }
  red[g][0][c] = a0; red[g][1][c] = a1;
  __syncthreads();
  if (threadIdx.x < 16) {
    int b = threadIdx.x >> 3; float s = 0;
    for (int i = 0; i < 32; ++i) s += red[i][b][c];
    float y = rbf(s);
    out[b * DM + j] = f2bf(x1[b * DM + j] + rbf(mod[b * NMOD + 5 * DM + j] * y));
  }
}

extern "C" void kernel_launch(void* const* d_in, const int* in_sizes, int n_in,
                              void* d_out, int out_size, void* d_ws, size_t ws_size,
                              hipStream_t stream) {
  const bf16_t* x = (const bf16_t*)d_in[0];
  const bf16_t* kc = (const bf16_t*)d_in[1];
  const bf16_t* vc = (const bf16_t*)d_in[2];
  const bf16_t* wq = (const bf16_t*)d_in[3];
  const bf16_t* wo = (const bf16_t*)d_in[4];
  const bf16_t* wg = (const bf16_t*)d_in[6];
  const bf16_t* wu = (const bf16_t*)d_in[7];
  const bf16_t* wd = (const bf16_t*)d_in[8];
  const bf16_t* c = (const bf16_t*)d_in[9];
  const bf16_t* wm = (const bf16_t*)d_in[10];
  char* ws = (char*)d_ws;
  float* mod = (float*)ws;
  bf16_t* q = (bf16_t*)(ws + 32768);
  float* pm = (float*)(ws + 65536);
  float* o = (float*)(ws + 98304);
  float* x1 = (float*)(ws + 131072);
  float* hh = (float*)(ws + 147456);
  float* po = (float*)(ws + 262144);
  k_mod<<<NMOD / 64, 256, 0, stream>>>(c, wm, mod);
  k_q<<<NH * DH / 64, 256, 0, stream>>>(x, mod, wq, q);
  k_attn<<<dim3(NSPLIT, NB * NH), 256, 0, stream>>>(q, kc, vc, pm, po);
  k_comb<<<NB * NH, 256, 0, stream>>>(pm, po, o);
  k_wo<<<DM / 8, 256, 0, stream>>>(o, wo, x, mod, x1);
  k_gu<<<FF / 16, 256, 0, stream>>>(x1, mod, wg, wu, hh);
  k_down<<<DM / 8, 256, 0, stream>>>(hh, wd, x1, mod, (bf16_t*)d_out);
}
```

```cpp
#include <hip/hip_runtime.h>
#include <stdint.h>

typedef unsigned short u16;
typedef unsigned long long u64;
typedef unsigned u32x4 __attribute__((ext_vector_type(4)));
typedef unsigned u32x2 __attribute__((ext_vector_type(2)));
typedef float f32x4 __attribute__((ext_vector_type(4)));
typedef float f32x2 __attribute__((ext_vector_type(2)));
typedef __bf16 bf16x2 __attribute__((ext_vector_type(2)));
typedef __bf16 bf16x8 __attribute__((ext_vector_type(8)));
#define GAS __attribute__((address_space(1)))
#define RLX_AGENT __ATOMIC_RELAXED, __HIP_MEMORY_SCOPE_AGENT
#define FENCE() asm volatile("" ::: "memory")
#define WAITVM(n) asm volatile("s_waitcnt vmcnt(" #n ")" ::: "memory")

constexpr int DM = 448, NH = 16, SK = 8192, DH = 256, FF = 1792, NMOD = 6 * DM, HD = NH * DH;
constexpr int UPH = SK / 8;
constexpr int NPRE = 5;
constexpr int SPIN = 1 << 20;
constexpr unsigned SENT = 0xFFFFFFFFu;
constexpr float CEXP = 0.0625f * 1.4426950408889634f;

constexpr unsigned WS_NONCE = 0;
constexpr unsigned WS_CLR = 4096;
constexpr unsigned WS_CNT = WS_CLR;
constexpr unsigned WS_ML = WS_CNT + 32 * 256;
constexpr unsigned WS_X1 = WS_ML + 2048;
constexpr unsigned WS_A1 = WS_X1 + 2048;
constexpr unsigned WS_A2 = WS_A1 + 16 * 16 * 1024;
constexpr unsigned WS_HH = WS_A2 + 7 * 8192;
constexpr unsigned WS_CLR_END = WS_CLR + 56 * 6144;
static_assert(WS_HH + 2 * FF * 2 <= WS_CLR_END, "cleared region too small");
constexpr unsigned WS_MODG = 352256;
constexpr unsigned WS_QG = WS_MODG + 8 * NMOD * 8;
constexpr unsigned WS_END = WS_QG + 32 * 8 * 1024;
static_assert(WS_CLR_END <= WS_MODG, "overlap");
#define STAMP(i) do { } while (0)

struct Params { const u16 *x, *kc, *vc, *wq, *wo, *wg, *wu, *wd, *c, *wm; u16* out; unsigned char* ws; };

__device__ __forceinline__ float bflo(unsigned v) { return __uint_as_float(v << 16); }
__device__ __forceinline__ float bfhi(unsigned v) { return __uint_as_float(v & 0xffff0000u); }
__device__ __forceinline__ unsigned pk2(float lo, float hi) { bf16x2 r = {(__bf16)lo, (__bf16)hi}; return __builtin_bit_cast(unsigned, r); }
__device__ __forceinline__ float rbf(float f) { return (float)(__bf16)f; }
__device__ __forceinline__ float dot2(unsigned a, unsigned b, float acc) {
  return __builtin_amdgcn_fdot2_f32_bf16(__builtin_bit_cast(bf16x2, a), __builtin_bit_cast(bf16x2, b), acc, false);
}
template <int CTRL> __device__ __forceinline__ float dpp(float x) {
  return __builtin_bit_cast(float, __builtin_amdgcn_mov_dpp(__builtin_bit_cast(int, x), CTRL, 0xf, 0xf, true));
}
constexpr int XOR1 = 0xB1, XOR2 = 0x4E, HMIR = 0x141, ROR8 = 0x128, BC0 = 0x150, BC8 = 0x158;
__device__ __forceinline__ float swap16_add(float a, float b) {
  auto s = __builtin_amdgcn_permlane16_swap(__float_as_uint(a), __float_as_uint(b), false, false);
  return __uint_as_float(s[0]) + __uint_as_float(s[1]);
}
__device__ __forceinline__ float swap32_add(float a, float b) {
  auto s = __builtin_amdgcn_permlane32_swap(__float_as_uint(a), __float_as_uint(b), false, false);
  return __uint_as_float(s[0]) + __uint_as_float(s[1]);
}
__device__ __forceinline__ float sum8(float v) { v += dpp<XOR1>(v); v += dpp<XOR2>(v); v += dpp<HMIR>(v); return v; }
__device__ __forceinline__ float sum16(float v) { v = sum8(v); v += dpp<ROR8>(v); return v; }
__device__ __forceinline__ float sum64(float v) { v = sum16(v); v = swap16_add(v, v); return swap32_add(v, v); }
__device__ __forceinline__ float sigm(float v) { return __builtin_amdgcn_rcpf(1.f + __builtin_amdgcn_exp2f(-1.4426950408889634f * v)); }
__device__ __forceinline__ f32x4 mfma(u32x4 a, u32x4 b, f32x4 c) {
  return __builtin_amdgcn_mfma_f32_16x16x32_bf16(__builtin_bit_cast(bf16x8, a), __builtin_bit_cast(bf16x8, b), c, 0, 0, 0);
}
__device__ __forceinline__ unsigned pair_lo(unsigned r0, unsigned r1) { return __builtin_amdgcn_perm(r1, r0, 0x05040100u); }
__device__ __forceinline__ unsigned pair_hi(unsigned r0, unsigned r1) { return __builtin_amdgcn_perm(r1, r0, 0x07060302u); }
__device__ __forceinline__ unsigned nosent(unsigned v) { return min(v, SENT - 1u); }
__device__ __forceinline__ unsigned mx4(u32x4 v) { return max(max(v[0], v[1]), max(v[2], v[3])); }
__device__ __forceinline__ bool ok4(u32x4 v) { return mx4(v) != SENT; }
__device__ __forceinline__ u64 ldg(const unsigned char* ws, unsigned off) { return __hip_atomic_load((const GAS u64*)(ws + off), RLX_AGENT); }
__device__ __forceinline__ void stg(unsigned char* ws, unsigned off, unsigned v, unsigned tag) {
  __hip_atomic_store((GAS u64*)(ws + off), ((u64)tag << 32) | v, RLX_AGENT);
}

typedef __amdgpu_buffer_rsrc_t rsrc_t;
__device__ __forceinline__ rsrc_t mkrs(const void* base, unsigned bytes) { return __builtin_amdgcn_make_buffer_rsrc((void*)base, 0, bytes, 0x00020000); }
constexpr int NT = 2, SC1 = 16;
#define KEEP(x) asm volatile("" : "+v"(x))
#define PIN() do { FENCE(); __builtin_amdgcn_sched_barrier(0); } while (0)
struct Unit { u32x4 k[4], v[4]; };
__device__ __forceinline__ void ld4nt(u32x4 (&d)[4], rsrc_t r, unsigned soff, unsigned voff) {
#pragma unroll
  for (int t = 0; t < 4; ++t) d[t] = __builtin_amdgcn_raw_buffer_load_b128(r, voff + t * 1024, soff, NT);
}
struct Att { float m, l; f32x2 acc[4]; };

template <bool REFILL>
__device__ __forceinline__ void attend(Unit& u, const u32x4 q, Att& a, const bool b3, rsrc_t kr, rsrc_t vr, unsigned soff, unsigned voff) {
  float s[4];
#pragma unroll
  for (int t = 0; t < 4; ++t) {
    float d = dot2(u.k[t][0], q[0], 0.f);
    d = dot2(u.k[t][1], q[1], d); d = dot2(u.k[t][2], q[2], d); s[t] = dot2(u.k[t][3], q[3], d);
  }
  if (REFILL) { asm volatile("" : "+v"(s[0]), "+v"(s[1]), "+v"(s[2]), "+v"(s[3]) :: "memory"); ld4nt(u.k, kr, soff, voff); FENCE(); }
  const float s01 = swap16_add(s[0], s[1]), s23 = swap16_add(s[2], s[3]);
  float z = (b3 ? s23 : s01) + dpp<ROR8>(b3 ? s01 : s23);
  z = rbf(sum8(z));
  if (__any(z > a.m)) {
    float mx = fmaxf(z, dpp<ROR8>(z));
    auto e = __builtin_amdgcn_permlane16_swap(__float_as_uint(mx), __float_as_uint(mx), false, false);
    mx = fmaxf(__uint_as_float(e[0]), __uint_as_float(e[1]));
    auto f = __builtin_amdgcn_permlane32_swap(__float_as_uint(mx), __float_as_uint(mx), false, false);
    mx = fmaxf(fmaxf(__uint_as_float(f[0]), __uint_as_float(f[1])), a.m);
    const float al = __builtin_amdgcn_exp2f((a.m - mx) * CEXP);
    a.m = mx; a.l *= al;
#pragma unroll
    for (int k = 0; k < 4; ++k) a.acc[k] *= al;
  }
  const float pz = __builtin_amdgcn_exp2f((z - a.m) * CEXP);
  a.l += pz;
  auto e = __builtin_amdgcn_permlane16_swap(__float_as_uint(pz), __float_as_uint(pz), false, false);
  const float pe = __uint_as_float(e[0]), po = __uint_as_float(e[1]);
  const float p[4] = {dpp<BC0>(pe), dpp<BC0>(po), dpp<BC8>(pe), dpp<BC8>(po)};
#pragma unroll
  for (int t = 0; t < 4; ++t)
#pragma unroll
    for (int k = 0; k < 4; ++k) {
      const f32x2 vv = {bflo(u.v[t][k]), bfhi(u.v[t][k])};
      a.acc[k] = __builtin_elementwise_fma(vv, f32x2{p[t], p[t]}, a.acc[k]);
    }
  asm volatile("" : "+v"(a.acc[0]), "+v"(a.acc[1]), "+v"(a.acc[2]), "+v"(a.acc[3]) :: "memory");
  if (REFILL) { ld4nt(u.v, vr, soff, voff); FENCE(); }
}

__device__ __forceinline__ void stream_and_tail(const int role, const Params& p, float* lds, Unit& ua, const u32x4 q, Att& at, unsigned tk, GAS unsigned* cnt, const rsrc_t kr, const rsrc_t vr,
                                                const rsrc_t wsr, const unsigned tagv, const int wh, const int wj
) {
  const int tid = threadIdx.x, lane = tid & 63, w = __builtin_amdgcn_readfirstlane(tid >> 6);
  const int wg = blockIdx.x, xg = wg & 7, slot = wg >> 3;
  const int g = lane >> 4, n = lane & 15;
  const bool b3 = lane & 8;
  const unsigned voff = lane * 16;
  unsigned char* const ws = p.ws;
  unsigned Ww[16], Wm[64], Wd[56];
  PIN();
  if (role == 1) {
    const rsrc_t wr = mkrs(p.wo, HD * DM * 2);
#pragma unroll
    for (int j = 0; j < 8; ++j) {
      const u32x2 t = __builtin_amdgcn_raw_buffer_load_b64(wr, (8 * g * DM + 4 * n) * 2, ((wh * DH + 32 * w + j) * DM + 64 * wj) * 2, NT);
      Ww[2 * j] = t[0]; Ww[2 * j + 1] = t[1];
    }
  }
  PIN();
  if (role == 2) {
    {
      const rsrc_t r0 = mkrs(p.wg, w < 7 ? DM * FF * 2 : 0), r1 = mkrs(p.wu, w < 7 ? DM * FF * 2 : 0);
#pragma unroll
      for (int s = 0; s < 2; ++s)
#pragma unroll
        for (int j = 0; j < 8; ++j) {
          const int o = ((64 * w + 32 * s + j) * FF + 64 * slot) * 2;
          const u32x2 t0 = __builtin_amdgcn_raw_buffer_load_b64(r0, (8 * g * FF + 4 * n) * 2, o, NT);
          const u32x2 t1 = __builtin_amdgcn_raw_buffer_load_b64(r1, (8 * g * FF + 4 * n) * 2, o, NT);
          Wm[s * 16 + 2 * j] = t0[0]; Wm[s * 16 + 2 * j + 1] = t0[1]; Wm[32 + s * 16 + 2 * j] = t1[0]; Wm[32 + s * 16 + 2 * j + 1] = t1[1];
        }
    }
  }
  PIN();
  if (role == 3) {
    const rsrc_t wr = mkrs(p.wd, FF * DM * 2);
#pragma unroll
    for (int s = 0; s < 7; ++s)
#pragma unroll
      for (int j = 0; j < 8; ++j) Wd[s * 8 + j] = __builtin_amdgcn_raw_buffer_load_b16(wr, (8 * g * DM + n) * 2, ((224 * w + 32 * s + j) * DM + 16 * slot) * 2, 0);
  }
  PIN();

  unsigned nx;
#pragma nounroll
  do {
    nx = min(NPRE * 64 + 1u + (unsigned)__builtin_amdgcn_readfirstlane(tk), (unsigned)UPH);
    if (lane == 0 && nx < (unsigned)UPH) tk = __hip_atomic_fetch_add(cnt, 1u, RLX_AGENT);
    attend<true>(ua, q, at, b3, kr, vr, nx * 4096u, voff);
  } while (nx < (unsigned)UPH);
  STAMP(4);

  {
    f32x4 o;
    o[0] = swap32_add(at.acc[0][0], at.acc[2][0]); o[1] = swap32_add(at.acc[0][1], at.acc[2][1]);
    o[2] = swap32_add(at.acc[1][0], at.acc[3][0]); o[3] = swap32_add(at.acc[1][1], at.acc[3][1]);
    float l = at.l + dpp<ROR8>(at.l);
    l = swap16_add(l, l); l = swap32_add(l, l);
    float* mg = lds + 2048;
    *(f32x4*)(mg + w * 256 + (lane & 31) * 8 + (lane >> 5) * 4) = o;
    if (lane == 0) *(f32x2*)(mg + 2048 + w * 2) = f32x2{at.m, l};
    __syncthreads();
    if (w == 0) {
      const f32x2 ml = *(const f32x2*)(mg + 2048 + (lane & 7) * 2);
      f32x4 pr[8];
#pragma unroll
      for (int i = 0; i < 8; ++i) pr[i] = *(const f32x4*)(mg + i * 256 + lane * 4);
      float M = fmaxf(ml[0], dpp<XOR1>(ml[0])); M = fmaxf(M, dpp<XOR2>(M)); M = fmaxf(M, dpp<HMIR>(M));
      const float wt = __builtin_amdgcn_exp2f((ml[0] - M) * CEXP);
      const float L = sum8(wt * ml[1]);
      f32x4 s = pr[0] * dpp<BC0>(wt);
      s += pr[1] * dpp<BC0 + 1>(wt); s += pr[2] * dpp<BC0 + 2>(wt); s += pr[3] * dpp<BC0 + 3>(wt);
      s += pr[4] * dpp<BC0 + 4>(wt); s += pr[5] * dpp<BC0 + 5>(wt); s += pr[6] * dpp<BC0 + 6>(wt); s += pr[7] * dpp<BC0 + 7>(wt);
      const int row = (slot & 15) * 16 + (slot >> 4) * 8 + xg;
      __builtin_amdgcn_raw_buffer_store_b128(u32x4{nosent(__float_as_uint(s[0])), nosent(__float_as_uint(s[1])), nosent(__float_as_uint(s[2])), nosent(__float_as_uint(s[3]))},
                                             wsr, WS_A1 + row * 1024 + lane * 16, 0, 16);
      if (lane == 0) __builtin_amdgcn_raw_buffer_store_b64(u32x2{nosent(__float_as_uint(M)), nosent(__float_as_uint(L))}, wsr, WS_ML + row * 8, 0, 16);
    }
  }
  STAMP(5);

  if (role == 1) {
    u32x4 bf[4];
#pragma unroll
    for (int i = 0; i < 4; ++i) {
      bf[0][i] = pair_lo(Ww[4 * i], Ww[4 * i + 2]); bf[1][i] = pair_hi(Ww[4 * i], Ww[4 * i + 2]);
      bf[2][i] = pair_lo(Ww[4 * i + 1], Ww[4 * i + 3]); bf[3][i] = pair_hi(Ww[4 * i + 1], Ww[4 * i + 3]);
    }
#pragma unroll
    for (int i = 0; i < 4; ++i) KEEP(bf[i]);
    u32x4 v0, v1; u32x2 ml;
    const unsigned o1 = WS_A1 + (wh * 16 + n) * 1024 + w * 128 + g * 32;
#pragma nounroll
    for (int it = 0; it < SPIN; ++it) {
      FENCE();
      v0 = __builtin_amdgcn_raw_buffer_load_b128(wsr, o1, 0, 16);
      v1 = __builtin_amdgcn_raw_buffer_load_b128(wsr, o1 + 16, 0, 16);
      ml = __builtin_amdgcn_raw_buffer_load_b64(wsr, WS_ML + (wh * 16 + n) * 8, 0, 16);
      if (max(max(mx4(v0), mx4(v1)), max(ml[0], ml[1])) != SENT) break;
    }
    STAMP(6);
    const float m = __uint_as_float(ml[0]);
    float M = fmaxf(m, dpp<XOR1>(m)); M = fmaxf(M, dpp<XOR2>(M)); M = fmaxf(M, dpp<HMIR>(M));
    const float wt = __builtin_amdgcn_exp2f((m - M) * CEXP);
    const float sc = wt * __builtin_amdgcn_rcpf(sum8(wt * __uint_as_float(ml[1])));
    u32x4 af;
    af[0] = pk2(sum8(sc * __uint_as_float(v0[0])), sum8(sc * __uint_as_float(v0[1])));
    af[1] = pk2(sum8(sc * __uint_as_float(v0[2])), sum8(sc * __uint_as_float(v0[3])));
    af[2] = pk2(sum8(sc * __uint_as_float(v1[0])), sum8(sc * __uint_as_float(v1[1])));
    af[3] = pk2(sum8(sc * __uint_as_float(v1[2])), sum8(sc * __uint_as_float(v1[3])));
    f32x4 d[4];
#pragma unroll
    for (int c = 0; c < 4; ++c) d[c] = mfma(af, bf[c], f32x4{0, 0, 0, 0});
    if (!(g & 1)) *(f32x4*)(lds + w * 128 + (g >> 1) * 64 + n * 4) = f32x4{d[0][0], d[1][0], d[2][0], d[3][0]};
    __syncthreads();
    if (tid < 32) {
      f32x4 s = {0, 0, 0, 0};
#pragma unroll
      for (int i = 0; i < 8; ++i) s += *(const f32x4*)(lds + i * 128 + tid * 4);
      __builtin_amdgcn_raw_buffer_store_b128(u32x4{nosent(__float_as_uint(s[0])), nosent(__float_as_uint(s[1])), nosent(__float_as_uint(s[2])), nosent(__float_as_uint(s[3]))},
                                             wsr, WS_A2 + wj * 8192 + wh * 512 + tid * 16, 0, 16);
    }
    STAMP(7);
  } else if (role == 2) {
    if (w < 7) {
      const int b = g & 1, dcol = 64 * w + 4 * n + 2 * (g >> 1);
      const unsigned xw = *(const GAS unsigned*)(p.x + b * DM + dcol);
      u64 mg[6];
#pragma nounroll
      for (int it = 0; it < SPIN; ++it) {
        bool ok = true;
#pragma unroll
        for (int i = 0; i < 6; ++i) { mg[i] = ldg(ws, WS_MODG + (xg * NMOD + (2 + (i >> 1)) * DM + dcol + (i & 1)) * 8); ok &= (unsigned)(mg[i] >> 32) == tagv; }
        if (__all(ok)) break;
      }
      float md[6];
#pragma unroll
      for (int i = 0; i < 6; ++i) md[i] = b ? bfhi((unsigned)mg[i]) : bflo((unsigned)mg[i]);
      md[4] = rbf(1.f + md[4]); md[5] = rbf(1.f + md[5]);
      u32x4 bf[2][2][4];
#pragma unroll
      for (int mt = 0; mt < 2; ++mt)
#pragma unroll
        for (int s = 0; s < 2; ++s)
#pragma unroll
          for (int i = 0; i < 4; ++i) {
            const int o = mt * 32 + s * 16 + 4 * i;
            bf[mt][s][0][i] = pair_lo(Wm[o], Wm[o + 2]); bf[mt][s][1][i] = pair_hi(Wm[o], Wm[o + 2]);
            bf[mt][s][2][i] = pair_lo(Wm[o + 1], Wm[o + 3]); bf[mt][s][3][i] = pair_hi(Wm[o + 1], Wm[o + 3]);
          }
#pragma unroll
      for (int i = 0; i < 16; ++i) KEEP(bf[i >> 3][(i >> 2) & 1][i & 3]);
#pragma unroll
      for (int i = 0; i < 6; ++i) KEEP(md[i]);
      u32x4 v[8];
#pragma unroll
      for (int i = 0; i < 8; ++i) v[i] = u32x4{SENT, SENT, SENT, SENT};
      {
        const unsigned char* pb = ws + WS_A2 + w * 8192 + 4096;
        unsigned pend = 0xff;
#pragma nounroll
        for (int it = 0; it < SPIN && pend; ++it) {
#define PLD(i) if (pend & (1u << i)) asm volatile("global_load_dwordx4 %0, %1, %2 offset:%3 sc1" : "+v"(v[i]) : "v"(voff), "s"(pb), "i"(i * 1024 - 4096) : "memory");
          PLD(0) PLD(1) PLD(2) PLD(3) PLD(4) PLD(5) PLD(6) PLD(7)
#undef PLD
          asm volatile("s_waitcnt vmcnt(0)" : "+v"(v[0]), "+v"(v[1]), "+v"(v[2]), "+v"(v[3]), "+v"(v[4]), "+v"(v[5]), "+v"(v[6]), "+v"(v[7]) :: "memory");
#pragma unroll
          for (int i = 0; i < 8; ++i) if (pend & (1u << i)) { if (__all(ok4(v[i]))) pend &= ~(1u << i); }
        }
      }
      STAMP(6);
      f32x4 s4 = __builtin_bit_cast(f32x4, v[0]);
#pragma unroll
      for (int i = 1; i < 8; ++i) s4 += __builtin_bit_cast(f32x4, v[i]);
      const float a0 = rbf(swap32_add(s4[0], s4[2])), a1 = rbf(swap32_add(s4[1], s4[3]));
      const unsigned x1p = pk2(bflo(xw) + rbf(md[0] * a0), bfhi(xw) + rbf(md[1] * a1));
      const float x0 = bflo(x1p), x1 = bfhi(x1p);
      if (slot == 0) __builtin_amdgcn_raw_buffer_store_b32(nosent(x1p), wsr, WS_X1 + (b * DM + dcol) * 2, 0, 16);
      float sm = sum16(x0 + x1); sm = swap32_add(sm, sm) * (1.f / 64);
      float sq = sum16((x0 - sm) * (x0 - sm) + (x1 - sm) * (x1 - sm)); sq = swap32_add(sq, sq);
      if (n == 0 && g < 2) *(f32x2*)(lds + 1536 + b * 16 + w * 2) = f32x2{sm, sq};
      __syncthreads();
      const f32x2 st = *(const f32x2*)(lds + 1536 + b * 16 + min(n & 7, 6) * 2);
      const bool live = (n & 7) < 7;
      const float mean = sum8(live ? st[0] : 0.f) * (1.f / 7);
      const float var = sum8(live ? st[1] + 64.f * (st[0] - mean) * (st[0] - mean) : 0.f) * (1.f / DM);
      const float rden = __builtin_amdgcn_rcpf(rbf(__builtin_sqrtf(rbf(rbf(var) + 1e-5f))));
      const float mb = rbf(mean);
      const unsigned hp = pk2(rbf(rbf(rbf(x0 - mb) * rden) * md[4]) + md[2], rbf(rbf(rbf(x1 - mb) * rden) * md[5]) + md[3]);
      unsigned* ht = (unsigned*)(lds + 1024 + w * 64);
      ht[b * 32 + 2 * n + (g >> 1)] = hp;
      FENCE();
      f32x4 d[2][4] = {};
#pragma unroll
      for (int s = 0; s < 2; ++s) {
        const u32x4 af = *(const u32x4*)(ht + (n & 1) * 32 + 16 * s + 4 * g);
#pragma unroll
        for (int mt = 0; mt < 2; ++mt)
#pragma unroll
          for (int c = 0; c < 4; ++c) d[mt][c] = mfma(af, bf[mt][s][c], d[mt][c]);
      }
      if (lane < 16) {
        float* r = lds + 4352 + w * 256 + n * 4;
#pragma unroll
        for (int mt = 0; mt < 2; ++mt)
#pragma unroll
          for (int bb = 0; bb < 2; ++bb) *(f32x4*)(r + mt * 128 + bb * 64) = f32x4{d[mt][0][bb], d[mt][1][bb], d[mt][2][bb], d[mt][3][bb]};
      }
    } else {
      __syncthreads();
    }
    __syncthreads();
    if (w == 0) {
      f32x2 gg = {0, 0}, uu = {0, 0};
#pragma unroll
      for (int i = 0; i < 7; ++i) { gg += *(const f32x2*)(lds + 4352 + i * 256 + lane * 2); uu += *(const f32x2*)(lds + 4352 + i * 256 + 128 + lane * 2); }
      const unsigned hh = pk2(rbf(sigm(rbf(gg[0]))) * rbf(uu[0]), rbf(sigm(rbf(gg[1]))) * rbf(uu[1]));
      __builtin_amdgcn_raw_buffer_store_b32(nosent(hh), wsr, WS_HH + (lane >> 5) * (FF * 2) + slot * 128 + (lane & 31) * 4, 0, 16);
    }
    STAMP(7);
  } else if (role == 3) {
    u32x4 bf[7];
#pragma unroll
    for (int s = 0; s < 7; ++s)
#pragma unroll
      for (int i = 0; i < 4; ++i) bf[s][i] = pair_lo(Wd[s * 8 + 2 * i], Wd[s * 8 + 2 * i + 1]);
#pragma unroll
    for (int i = 0; i < 7; ++i) KEEP(bf[i]);
    float gb = 0.f, x1 = 0.f;
    if (w == 0) {
      const int b = g & 1, col = 16 * slot + n;
      u64 gg;
#pragma nounroll
      for (int it = 0; it < SPIN; ++it) { gg = ldg(ws, WS_MODG + (xg * NMOD + 5 * DM + col) * 8); if (__all((unsigned)(gg >> 32) == tagv)) break; }
      gb = b ? bfhi((unsigned)gg) : bflo((unsigned)gg);
      unsigned xw;
#pragma nounroll
      for (int it = 0; it < SPIN; ++it) { FENCE(); xw = __builtin_amdgcn_raw_buffer_load_b32(wsr, WS_X1 + (b * DM + (col & ~1)) * 2, 0, 16); if (__all(xw != SENT)) break; }
      x1 = (col & 1) ? bfhi(xw) : bflo(xw);
    }
    u32x4 af = {0, 0, 0, 0};
    if (n < 14) {
      const unsigned o3 = WS_HH + (n & 1) * (FF * 2) + (224 * w + 32 * (n >> 1) + 8 * g) * 2;
#pragma nounroll
      for (int it = 0; it < SPIN; ++it) { FENCE(); af = __builtin_amdgcn_raw_buffer_load_b128(wsr, o3, 0, 16); if (ok4(af)) break; }
    }
    STAMP(6);
    f32x4 d[7];
#pragma unroll
    for (int s = 0; s < 7; ++s) d[s] = mfma(af, bf[s], f32x4{0, 0, 0, 0});
    f32x2 t0 = {d[0][0] + d[1][2], d[0][1] + d[1][3]}, t1 = {d[2][0] + d[3][2], d[2][1] + d[3][3]}, t2 = {d[4][0] + d[5][2], d[4][1] + d[5][3]};
    KEEP(t0); KEEP(t1); KEEP(t2);
    const bool g0 = g & 1, g1 = g & 2;
    const float y0 = g1 ? (g0 ? d[6][0] : t2[0]) : (g0 ? t1[0] : t0[0]);
    const float y1 = g1 ? (g0 ? d[6][1] : t2[1]) : (g0 ? t1[1] : t0[1]);
    float y = swap16_add(y0, y1); y = swap32_add(y, y);
    if (lane < 32) lds[w * 32 + lane] = y;
    __syncthreads();
    if (tid < 32) {
      float s = 0;
#pragma unroll
      for (int i = 0; i < 8; ++i) s += lds[i * 32 + tid];
      ((GAS u16*)p.out)[g * DM + 16 * slot + n] = (u16)pk2(x1 + rbf(gb * rbf(s)), 0.f);
      if (tid == 0 && slot == 0) __hip_atomic_store((GAS unsigned*)(ws + WS_NONCE), tagv, RLX_AGENT);
    }
    STAMP(7);
  }
}

__global__ void __launch_bounds__(512, 2) fused_block(Params p) {
  __shared__ __attribute__((aligned(16))) float lds[8192];
  const int tid = threadIdx.x, lane = tid & 63, w = __builtin_amdgcn_readfirstlane(tid >> 6);
  const int wg = blockIdx.x, xg = wg & 7, slot = wg >> 3;
  const int g = lane >> 4, n = lane & 15;
  unsigned char* const ws = p.ws;
  const rsrc_t wsr = mkrs(ws, WS_END);
  STAMP(0);
  const unsigned nonce = __hip_atomic_load((const GAS unsigned*)(ws + WS_NONCE), RLX_AGENT);
#define GET_TAG() ({ unsigned t_; asm volatile("v_mov_b32 %0, %1" : "=v"(t_) : "v"(nonce)); (unsigned)__builtin_amdgcn_readfirstlane(t_) + 1u; })

  const int apair = ((slot - 8) >> 1) * 8 + xg;
  if (slot >= 8 && slot < 20 && apair < 42) {
    const int a = 2 * apair + (slot & 1);
    if (w < 7) {
      unsigned W[2][8]; u32x4 cv[2];
#pragma unroll
      for (int s = 0; s < 2; ++s) cv[s] = *(const GAS u32x4*)(p.c + (n & 1) * DM + 64 * w + 32 * s + 8 * g);
      const rsrc_t wr = mkrs(p.wm, DM * NMOD * 2);
#pragma unroll
      for (int s = 0; s < 2; ++s)
#pragma unroll
        for (int j = 0; j < 8; ++j) W[s][j] = __builtin_amdgcn_raw_buffer_load_b32(wr, (8 * g * NMOD + 2 * n) * 2, ((64 * w + 32 * s + j) * NMOD + 32 * a) * 2, 0);
      PIN();
      f32x4 d0 = {0, 0, 0, 0}, d1 = {0, 0, 0, 0};
#pragma unroll
      for (int s = 0; s < 2; ++s) {
        u32x4 af, b0, b1;
#pragma unroll
        for (int i = 0; i < 4; ++i) {
          const float lo = bflo(cv[s][i]), hi = bfhi(cv[s][i]);
          af[i] = pk2(lo * sigm(lo), hi * sigm(hi));
          b0[i] = pair_lo(W[s][2 * i], W[s][2 * i + 1]); b1[i] = pair_hi(W[s][2 * i], W[s][2 * i + 1]);
        }
        d0 = mfma(af, b0, d0); d1 = mfma(af, b1, d1);
      }
      if (lane < 16) *(f32x4*)(lds + w * 64 + n * 4) = f32x4{d0[0], d0[1], d1[0], d1[1]};
    } else if (a >= 28) {
#pragma unroll
      for (int i = 0; i < 6; ++i) __builtin_amdgcn_raw_buffer_store_b128(u32x4{SENT, SENT, SENT, SENT}, wsr, WS_CLR + (a - 28) * 6144 + i * 1024 + lane * 16, 0, 16);
      WAITVM(0);
    }
    __syncthreads();
    const unsigned tagv = GET_TAG();
    if (tid < 256) {
      const int cc = tid & 31, rep = tid >> 5;
      f32x2 s = {0, 0};
#pragma unroll
      for (int i = 0; i < 7; ++i) s += *(const f32x2*)(lds + i * 64 + cc * 2);
      stg(ws, WS_MODG + (rep * NMOD + 32 * a + cc) * 8, pk2(s[0], s[1]), tagv);
    }
    STAMP(1);
  }
  else if (slot < 8) {
    const int bq = slot * 8 + xg;
    if (w < 7) {
      u32x2 W[2][8]; u32x4 xa[2]; unsigned xo[2];
#pragma unroll
      for (int b = 0; b < 2; ++b) {
        xa[b] = __builtin_amdgcn_raw_buffer_load_b128(mkrs(p.x + b * DM, DM * 2), lane * 16, 0, 0);
        xo[b] = ((const GAS u16*)p.x)[b * DM + 64 * w + lane];
      }
      const rsrc_t wr = mkrs(p.wq, DM * HD * 2);
#pragma unroll
      for (int s = 0; s < 2; ++s)
#pragma unroll
        for (int j = 0; j < 8; ++j) W[s][j] = __builtin_amdgcn_raw_buffer_load_b64(wr, (8 * g * HD + 4 * n) * 2, ((64 * w + 32 * s + j) * HD + 64 * bq) * 2, NT);
      PIN();
      float nrm[2];
#pragma unroll
      for (int b = 0; b < 2; ++b) {
        float s = 0;
#pragma unroll
        for (int i = 0; i < 4; ++i) s += bflo(xa[b][i]) + bfhi(xa[b][i]);
        const float mean = sum64(s) * (1.f / DM);
        float v = 0;
#pragma unroll
        for (int i = 0; i < 4; ++i) { const float lo = bflo(xa[b][i]) - mean, hi = bfhi(xa[b][i]) - mean; v += lo * lo + hi * hi; }
        const float den = rbf(__builtin_sqrtf(rbf(rbf(sum64(lane < 56 ? v : 0.f) * (1.f / DM)) + 1e-5f)));
        nrm[b] = rbf(rbf(bflo(xo[b]) - rbf(mean)) / den);
      }
      u32x4 bf[2][4];
#pragma unroll
      for (int s = 0; s < 2; ++s)
#pragma unroll
        for (int i = 0; i < 4; ++i) {
          bf[s][0][i] = pair_lo(W[s][2 * i][0], W[s][2 * i + 1][0]); bf[s][1][i] = pair_hi(W[s][2 * i][0], W[s][2 * i + 1][0]);
          bf[s][2][i] = pair_lo(W[s][2 * i][1], W[s][2 * i + 1][1]); bf[s][3][i] = pair_hi(W[s][2 * i][1], W[s][2 * i + 1][1]);
        }
      const unsigned tagv = GET_TAG();
      KEEP(nrm[0]); KEEP(nrm[1]);
#pragma unroll
      for (int i = 0; i < 8; ++i) KEEP(bf[i >> 2][i & 3]);
      PIN();
      u64 gs, gc;
#pragma nounroll
      for (int it = 0; it < SPIN; ++it) {
        gs = ldg(ws, WS_MODG + (xg * NMOD + 64 * w + lane) * 8);
        gc = ldg(ws, WS_MODG + (xg * NMOD + DM + 64 * w + lane) * 8);
        if (__all((unsigned)(gs >> 32) == tagv && (unsigned)(gc >> 32) == tagv)) break;
      }
      STAMP(1);
      const float h0 = rbf(nrm[0] * rbf(1.f + bflo((unsigned)gc))) + bflo((unsigned)gs);
      const float h1 = rbf(nrm[1] * rbf(1.f + bfhi((unsigned)gc))) + bfhi((unsigned)gs);
      const unsigned hp = pk2(h0, h1);
      u16* ht = (u16*)(lds + 1024 + w * 64);
      ht[lane] = (u16)hp; ht[64 + lane] = (u16)(hp >> 16);
      FENCE();
      f32x4 d[4] = {};
#pragma unroll
      for (int s = 0; s < 2; ++s) {
        const u32x4 af = *(const u32x4*)(ht + (n & 1) * 64 + 32 * s + 8 * g);
#pragma unroll
        for (int c = 0; c < 4; ++c) d[c] = mfma(af, bf[s][c], d[c]);
      }
      if (lane < 16) {
        *(f32x4*)(lds + w * 128 + n * 4) = f32x4{d[0][0], d[1][0], d[2][0], d[3][0]};
        *(f32x4*)(lds + w * 128 + 64 + n * 4) = f32x4{d[0][1], d[1][1], d[2][1], d[3][1]};
      }
    }
    __syncthreads();
    {
      const unsigned tagv = GET_TAG();
      f32x2 s = {0, 0};
#pragma unroll
      for (int i = 0; i < 7; ++i) s += *(const f32x2*)(lds + i * 128 + lane * 2);
      stg(ws, WS_QG + ((((lane >> 5) * NH + (bq >> 2)) * 8 + w) * 128 + (bq & 3) * 32 + (lane & 31)) * 8, pk2(s[0], s[1]), tagv);
    }
    STAMP(2);
  }

  const rsrc_t kr = mkrs(p.kc + (size_t)slot * (SK * DH), SK * DH * 2), vr = mkrs(p.vc + (size_t)slot * (SK * DH), SK * DH * 2);
  const unsigned voff = lane * 16;
  const unsigned u0 = (unsigned)(xg * (NPRE * 8) + w) * 4096u;
  Unit ua, ub, uc, ud;
  PIN();
  ld4nt(ua.k, kr, u0, voff); ld4nt(ua.v, vr, u0, voff);
  WAITVM(4); ld4nt(ub.k, kr, u0 + 32768, voff);
  WAITVM(4); ld4nt(ub.v, vr, u0 + 32768, voff);
  WAITVM(4); ld4nt(uc.k, kr, u0 + 65536, voff);
  WAITVM(4); ld4nt(uc.v, vr, u0 + 65536, voff);
  WAITVM(4);
  const unsigned tagv = GET_TAG();
  u64 qg[5];
  const unsigned qoff = WS_QG + ((slot * 8 + xg) * 128 + (lane & 31) * 4) * 8;
  const unsigned foff = WS_MODG + (xg * NMOD + 32 * (28 + min(lane, 55))) * 8;
#pragma unroll
  for (int i = 0; i < 4; ++i) qg[i] = ldg(ws, qoff + i * 8);
  qg[4] = ldg(ws, foff);
  FENCE();
  ld4nt(ud.k, kr, u0 + 98304, voff);
  WAITVM(9); ld4nt(ud.v, vr, u0 + 98304, voff);
  PIN();
#define TG(v) ((unsigned)((v) >> 32) ^ tagv)
#define Q_OK() __all((TG(qg[0]) | TG(qg[1]) | TG(qg[2]) | TG(qg[3]) | TG(qg[4])) == 0)
  if (!Q_OK()) {
#pragma nounroll
    for (int it = 0; it < SPIN; ++it) {
#pragma unroll
      for (int i = 0; i < 4; ++i) qg[i] = ldg(ws, qoff + i * 8);
      qg[4] = ldg(ws, foff);
      if (Q_OK()) break;
    }
  }
  STAMP(3);
  const u32x4 q = {(unsigned)qg[0], (unsigned)qg[1], (unsigned)qg[2], (unsigned)qg[3]};
  unsigned lz = lane;
  asm volatile("" : "+v"(lz));
  GAS unsigned* const cnt = (GAS unsigned*)(ws + WS_CNT + slot * 256) + lz;
  unsigned tk = 0;
  if (lane == 0) tk = __hip_atomic_fetch_add(cnt, 1u, RLX_AGENT);
  const bool b3 = lane & 8;
  Att at; at.m = -INFINITY; at.l = 0.f;
#pragma unroll
  for (int k = 0; k < 4; ++k) at.acc[k] = f32x2{0.f, 0.f};
  attend<true>(ua, q, at, b3, kr, vr, u0 + 131072, voff);
  attend<false>(ub, q, at, b3, kr, vr, 0, 0);
  attend<false>(uc, q, at, b3, kr, vr, 0, 0);
  attend<false>(ud, q, at, b3, kr, vr, 0, 0);

  const int wh = (xg - 2) + 6 * (slot / 7), wj = slot % 7;
  const int role = (xg == 0 && slot < 28) ? 2 : (xg == 1 && slot < 28) ? 3 : (xg >= 2 && slot < 21 && wh < 16) ? 1 : 0;
#define REST(r) stream_and_tail(r, p, lds, ua, q, at, tk, cnt, kr, vr, wsr, tagv, wh, wj)
  REST(role);
}

extern "C" void kernel_launch(void* const* d_in, const int* in_sizes, int n_in, void* d_out, int out_size, void* d_ws, size_t ws_size, hipStream_t stream) {
  Params p;
  p.x = (const u16*)d_in[0]; p.kc = (const u16*)d_in[1]; p.vc = (const u16*)d_in[2]; p.wq = (const u16*)d_in[3]; p.wo = (const u16*)d_in[4];
  p.wg = (const u16*)d_in[6]; p.wu = (const u16*)d_in[7]; p.wd = (const u16*)d_in[8]; p.c = (const u16*)d_in[9]; p.wm = (const u16*)d_in[10];
  p.out = (u16*)d_out; p.ws = (unsigned char*)d_ws;
  fused_block<<<256, 512, 0, stream>>>(p);
}
```

```cpp
#include <hip/hip_runtime.h>
#include <stdint.h>
#define STAMPS
#define PROBE 0

typedef unsigned short u16;
typedef unsigned long long u64;
typedef unsigned u32x4 __attribute__((ext_vector_type(4)));
typedef unsigned u32x2 __attribute__((ext_vector_type(2)));
typedef float f32x4 __attribute__((ext_vector_type(4)));
typedef float f32x2 __attribute__((ext_vector_type(2)));
typedef __bf16 bf16x2 __attribute__((ext_vector_type(2)));
typedef __bf16 bf16x8 __attribute__((ext_vector_type(8)));
#define GAS __attribute__((address_space(1)))
#define RLX_AGENT __ATOMIC_RELAXED, __HIP_MEMORY_SCOPE_AGENT
#define FENCE() asm volatile("" ::: "memory")
#define WAITVM(n) asm volatile("s_waitcnt vmcnt(" #n ")" ::: "memory")

constexpr int DM = 448, NH = 16, SK = 8192, DH = 256, FF = 1792, NMOD = 6 * DM, HD = NH * DH;
constexpr int UPH = SK / 8;
constexpr int NPRE = 5;
constexpr int SPIN = 1 << 20;
constexpr unsigned SENT = 0xFFFFFFFFu;
constexpr float CEXP = 0.0625f * 1.4426950408889634f;

constexpr unsigned WS_NONCE = 0;
constexpr unsigned WS_CLR = 4096;
constexpr unsigned WS_CNT = WS_CLR;
constexpr unsigned WS_ML = WS_CNT + 32 * 256;
constexpr unsigned WS_X1 = WS_ML + 2048;
constexpr unsigned WS_A1 = WS_X1 + 2048;
constexpr unsigned WS_A2 = WS_A1 + 16 * 16 * 1024;
constexpr unsigned WS_HH = WS_A2 + 7 * 8192;
constexpr unsigned WS_CLR_END = WS_CLR + 56 * 6144;
static_assert(WS_HH + 2 * FF * 2 <= WS_CLR_END, "cleared region too small");
constexpr unsigned WS_MODG = 352256;
constexpr unsigned WS_QG = WS_MODG + 8 * NMOD * 8;
constexpr unsigned WS_END = WS_QG + 32 * 8 * 1024;
static_assert(WS_CLR_END <= WS_MODG, "overlap");
constexpr unsigned WS_TS = 1u << 20;
#define STAMP(i) do { ts[i] = __builtin_amdgcn_s_memrealtime(); } while (0)

struct Params { const u16 *x, *kc, *vc, *wq, *wo, *wg, *wu, *wd, *c, *wm; u16* out; unsigned char* ws; };

__device__ __forceinline__ float bflo(unsigned v) { return __uint_as_float(v << 16); }
__device__ __forceinline__ float bfhi(unsigned v) { return __uint_as_float(v & 0xffff0000u); }
__device__ __forceinline__ unsigned pk2(float lo, float hi) { bf16x2 r = {(__bf16)lo, (__bf16)hi}; return __builtin_bit_cast(unsigned, r); }
__device__ __forceinline__ float rbf(float f) { return (float)(__bf16)f; }
__device__ __forceinline__ float dot2(unsigned a, unsigned b, float acc) {
  return __builtin_amdgcn_fdot2_f32_bf16(__builtin_bit_cast(bf16x2, a), __builtin_bit_cast(bf16x2, b), acc, false);
}
template <int CTRL> __device__ __forceinline__ float dpp(float x) {
  return __builtin_bit_cast(float, __builtin_amdgcn_mov_dpp(__builtin_bit_cast(int, x), CTRL, 0xf, 0xf, true));
}
constexpr int XOR1 = 0xB1, XOR2 = 0x4E, HMIR = 0x141, ROR8 = 0x128, BC0 = 0x150, BC8 = 0x158;
__device__ __forceinline__ float swap16_add(float a, float b) {
  auto s = __builtin_amdgcn_permlane16_swap(__float_as_uint(a), __float_as_uint(b), false, false);
  return __uint_as_float(s[0]) + __uint_as_float(s[1]);
}
__device__ __forceinline__ float swap32_add(float a, float b) {
  auto s = __builtin_amdgcn_permlane32_swap(__float_as_uint(a), __float_as_uint(b), false, false);
  return __uint_as_float(s[0]) + __uint_as_float(s[1]);
}
__device__ __forceinline__ float sum8(float v) { v += dpp<XOR1>(v); v += dpp<XOR2>(v); v += dpp<HMIR>(v); return v; }
__device__ __forceinline__ float sum16(float v) { v = sum8(v); v += dpp<ROR8>(v); return v; }
__device__ __forceinline__ float sum64(float v) { v = sum16(v); v = swap16_add(v, v); return swap32_add(v, v); }
__device__ __forceinline__ float sigm(float v) { return __builtin_amdgcn_rcpf(1.f + __builtin_amdgcn_exp2f(-1.4426950408889634f * v)); }
__device__ __forceinline__ f32x4 mfma(u32x4 a, u32x4 b, f32x4 c) {
  return __builtin_amdgcn_mfma_f32_16x16x32_bf16(__builtin_bit_cast(bf16x8, a), __builtin_bit_cast(bf16x8, b), c, 0, 0, 0);
}
__device__ __forceinline__ unsigned pair_lo(unsigned r0, unsigned r1) { return __builtin_amdgcn_perm(r1, r0, 0x05040100u); }
__device__ __forceinline__ unsigned pair_hi(unsigned r0, unsigned r1) { return __builtin_amdgcn_perm(r1, r0, 0x07060302u); }
__device__ __forceinline__ unsigned nosent(unsigned v) { return min(v, SENT - 1u); }
__device__ __forceinline__ unsigned mx4(u32x4 v) { return max(max(v[0], v[1]), max(v[2], v[3])); }
__device__ __forceinline__ bool ok4(u32x4 v) { return mx4(v) != SENT; }
__device__ __forceinline__ u64 ldg(const unsigned char* ws, unsigned off) { return __hip_atomic_load((const GAS u64*)(ws + off), RLX_AGENT); }
__device__ __forceinline__ void stg(unsigned char* ws, unsigned off, unsigned v, unsigned tag) {
  __hip_atomic_store((GAS u64*)(ws + off), ((u64)tag << 32) | v, RLX_AGENT);
}

typedef __amdgpu_buffer_rsrc_t rsrc_t;
__device__ __forceinline__ rsrc_t mkrs(const void* base, unsigned bytes) { return __builtin_amdgcn_make_buffer_rsrc((void*)base, 0, bytes, 0x00020000); }
constexpr int NT = 2, SC1 = 16;
#define KEEP(x) asm volatile("" : "+v"(x))
#define PIN() do { FENCE(); __builtin_amdgcn_sched_barrier(0); } while (0)
struct Unit { u32x4 k[4], v[4]; };
__device__ __forceinline__ void ld4nt(u32x4 (&d)[4], rsrc_t r, unsigned soff, unsigned voff) {
#pragma unroll
  for (int t = 0; t < 4; ++t) d[t] = __builtin_amdgcn_raw_buffer_load_b128(r, voff + t * 1024, soff, NT);
}
struct Att { float m, l; f32x2 acc[4]; };

template <bool REFILL>
__device__ __forceinline__ void attend(Unit& u, const u32x4 q, Att& a, const bool b3, rsrc_t kr, rsrc_t vr, unsigned soff, unsigned voff) {
  float s[4];
#pragma unroll
  for (int t = 0; t < 4; ++t) {
    float d = dot2(u.k[t][0], q[0], 0.f);
    d = dot2(u.k[t][1], q[1], d); d = dot2(u.k[t][2], q[2], d); s[t] = dot2(u.k[t][3], q[3], d);
  }
  if (REFILL) { asm volatile("" : "+v"(s[0]), "+v"(s[1]), "+v"(s[2]), "+v"(s[3]) :: "memory"); ld4nt(u.k, kr, soff, voff); FENCE(); }
  const float s01 = swap16_add(s[0], s[1]), s23 = swap16_add(s[2], s[3]);
  float z = (b3 ? s23 : s01) + dpp<ROR8>(b3 ? s01 : s23);
  z = rbf(sum8(z));
  if (__any(z > a.m)) {
    float mx = fmaxf(z, dpp<ROR8>(z));
    auto e = __builtin_amdgcn_permlane16_swap(__float_as_uint(mx), __float_as_uint(mx), false, false);
    mx = fmaxf(__uint_as_float(e[0]), __uint_as_float(e[1]));
    auto f = __builtin_amdgcn_permlane32_swap(__float_as_uint(mx), __float_as_uint(mx), false, false);
    mx = fmaxf(fmaxf(__uint_as_float(f[0]), __uint_as_float(f[1])), a.m);
    const float al = __builtin_amdgcn_exp2f((a.m - mx) * CEXP);
    a.m = mx; a.l *= al;
#pragma unroll
    for (int k = 0; k < 4; ++k) a.acc[k] *= al;
  }
  const float pz = __builtin_amdgcn_exp2f((z - a.m) * CEXP);
  a.l += pz;
  auto e = __builtin_amdgcn_permlane16_swap(__float_as_uint(pz), __float_as_uint(pz), false, false);
  const float pe = __uint_as_float(e[0]), po = __uint_as_float(e[1]);
  const float p[4] = {dpp<BC0>(pe), dpp<BC0>(po), dpp<BC8>(pe), dpp<BC8>(po)};
#pragma unroll
  for (int t = 0; t < 4; ++t)
#pragma unroll
    for (int k = 0; k < 4; ++k) {
      const f32x2 vv = {bflo(u.v[t][k]), bfhi(u.v[t][k])};
      a.acc[k] = __builtin_elementwise_fma(vv, f32x2{p[t], p[t]}, a.acc[k]);
    }
  asm volatile("" : "+v"(a.acc[0]), "+v"(a.acc[1]), "+v"(a.acc[2]), "+v"(a.acc[3]) :: "memory");
  if (REFILL) { ld4nt(u.v, vr, soff, voff); FENCE(); }
}

__device__ __forceinline__ void stream_and_tail(const int role, const Params& p, float* lds, Unit& ua, const u32x4 q, Att& at, unsigned tk, GAS unsigned* cnt, const rsrc_t kr, const rsrc_t vr,
                                                const rsrc_t wsr, const unsigned tagv, const int wh, const int wj
                                                , u64 (&ts)[12]
) {
  const int tid = threadIdx.x, lane = tid & 63, w = __builtin_amdgcn_readfirstlane(tid >> 6);
  const int wg = blockIdx.x, xg = wg & 7, slot = wg >> 3;
  const int g = lane >> 4, n = lane & 15;
  const bool b3 = lane & 8;
  const unsigned voff = lane * 16;
  unsigned char* const ws = p.ws;
  unsigned Ww[16], Wm[64], Wd[56];
  PIN();
  if (role == 1) {
    const rsrc_t wr = mkrs(p.wo, HD * DM * 2);
#pragma unroll
    for (int j = 0; j < 8; ++j) {
      const u32x2 t = __builtin_amdgcn_raw_buffer_load_b64(wr, (8 * g * DM + 4 * n) * 2, ((wh * DH + 32 * w + j) * DM + 64 * wj) * 2, NT);
      Ww[2 * j] = t[0]; Ww[2 * j + 1] = t[1];
    }
  }
  PIN();
  if (role == 2) {
    {
      const rsrc_t r0 = mkrs(p.wg, w < 7 ? DM * FF * 2 : 0), r1 = mkrs(p.wu, w < 7 ? DM * FF * 2 : 0);
#pragma unroll
      for (int s = 0; s < 2; ++s)
#pragma unroll
        for (int j = 0; j < 8; ++j) {
          const int o = ((64 * w + 32 * s + j) * FF + 64 * slot) * 2;
          const u32x2 t0 = __builtin_amdgcn_raw_buffer_load_b64(r0, (8 * g * FF + 4 * n) * 2, o, NT);
          const u32x2 t1 = __builtin_amdgcn_raw_buffer_load_b64(r1, (8 * g * FF + 4 * n) * 2, o, NT);
          Wm[s * 16 + 2 * j] = t0[0]; Wm[s * 16 + 2 * j + 1] = t0[1]; Wm[32 + s * 16 + 2 * j] = t1[0]; Wm[32 + s * 16 + 2 * j + 1] = t1[1];
        }
    }
  }
  PIN();
  if (role == 3) {
    const rsrc_t wr = mkrs(p.wd, FF * DM * 2);
#pragma unroll
    for (int s = 0; s < 7; ++s)
#pragma unroll
      for (int j = 0; j < 8; ++j) Wd[s * 8 + j] = __builtin_amdgcn_raw_buffer_load_b16(wr, (8 * g * DM + n) * 2, ((224 * w + 32 * s + j) * DM + 16 * slot) * 2, 0);
  }
  PIN();

  unsigned nx;
#pragma nounroll
  do {
    nx = min(NPRE * 64 + 1u + (unsigned)__builtin_amdgcn_readfirstlane(tk), (unsigned)UPH);
    if (lane == 0 && nx < (unsigned)UPH) tk = __hip_atomic_fetch_add(cnt, 1u, RLX_AGENT);
    attend<true>(ua, q, at, b3, kr, vr, nx * 4096u, voff);
  } while (nx < (unsigned)UPH);
  STAMP(4);

  {
    f32x4 o;
    o[0] = swap32_add(at.acc[0][0], at.acc[2][0]); o[1] = swap32_add(at.acc[0][1], at.acc[2][1]);
    o[2] = swap32_add(at.acc[1][0], at.acc[3][0]); o[3] = swap32_add(at.acc[1][1], at.acc[3][1]);
    float l = at.l + dpp<ROR8>(at.l);
    l = swap16_add(l, l); l = swap32_add(l, l);
    float* mg = lds + 2048;
    *(f32x4*)(mg + w * 256 + (lane & 31) * 8 + (lane >> 5) * 4) = o;
    if (lane == 0) *(f32x2*)(mg + 2048 + w * 2) = f32x2{at.m, l};
    __syncthreads();
    if (w == 0) {
      const f32x2 ml = *(const f32x2*)(mg + 2048 + (lane & 7) * 2);
      f32x4 pr[8];
#pragma unroll
      for (int i = 0; i < 8; ++i) pr[i] = *(const f32x4*)(mg + i * 256 + lane * 4);
      float M = fmaxf(ml[0], dpp<XOR1>(ml[0])); M = fmaxf(M, dpp<XOR2>(M)); M = fmaxf(M, dpp<HMIR>(M));
      const float wt = __builtin_amdgcn_exp2f((ml[0] - M) * CEXP);
      const float L = sum8(wt * ml[1]);
      f32x4 s = pr[0] * dpp<BC0>(wt);
      s += pr[1] * dpp<BC0 + 1>(wt); s += pr[2] * dpp<BC0 + 2>(wt); s += pr[3] * dpp<BC0 + 3>(wt);
      s += pr[4] * dpp<BC0 + 4>(wt); s += pr[5] * dpp<BC0 + 5>(wt); s += pr[6] * dpp<BC0 + 6>(wt); s += pr[7] * dpp<BC0 + 7>(wt);
      const int row = (slot & 15) * 16 + (slot >> 4) * 8 + xg;
      __builtin_amdgcn_raw_buffer_store_b128(u32x4{nosent(__float_as_uint(s[0])), nosent(__float_as_uint(s[1])), nosent(__float_as_uint(s[2])), nosent(__float_as_uint(s[3]))},
                                             wsr, WS_A1 + row * 1024 + lane * 16, 0, 16);
      if (lane == 0) __builtin_amdgcn_raw_buffer_store_b64(u32x2{nosent(__float_as_uint(M)), nosent(__float_as_uint(L))}, wsr, WS_ML + row * 8, 0, 16);
    }
  }
  STAMP(5);

  if (role == 1) {
    u32x4 bf[4];
#pragma unroll
    for (int i = 0; i < 4; ++i) {
      bf[0][i] = pair_lo(Ww[4 * i], Ww[4 * i + 2]); bf[1][i] = pair_hi(Ww[4 * i], Ww[4 * i + 2]);
      bf[2][i] = pair_lo(Ww[4 * i + 1], Ww[4 * i + 3]); bf[3][i] = pair_hi(Ww[4 * i + 1], Ww[4 * i + 3]);
    }
#pragma unroll
    for (int i = 0; i < 4; ++i) KEEP(bf[i]);
    u32x4 v0, v1; u32x2 ml;
    const unsigned o1 = WS_A1 + (wh * 16 + n) * 1024 + w * 128 + g * 32;
#pragma nounroll
    for (int it = 0; it < SPIN; ++it) {
      FENCE();
      v0 = __builtin_amdgcn_raw_buffer_load_b128(wsr, o1, 0, 16);
      v1 = __builtin_amdgcn_raw_buffer_load_b128(wsr, o1 + 16, 0, 16);
      ml = __builtin_amdgcn_raw_buffer_load_b64(wsr, WS_ML + (wh * 16 + n) * 8, 0, 16);
      if (max(max(mx4(v0), mx4(v1)), max(ml[0], ml[1])) != SENT) break;
    }
    STAMP(6);
    const float m = __uint_as_float(ml[0]);
    float M = fmaxf(m, dpp<XOR1>(m)); M = fmaxf(M, dpp<XOR2>(M)); M = fmaxf(M, dpp<HMIR>(M));
    const float wt = __builtin_amdgcn_exp2f((m - M) * CEXP);
    const float sc = wt * __builtin_amdgcn_rcpf(sum8(wt * __uint_as_float(ml[1])));
    u32x4 af;
    af[0] = pk2(sum8(sc * __uint_as_float(v0[0])), sum8(sc * __uint_as_float(v0[1])));
    af[1] = pk2(sum8(sc * __uint_as_float(v0[2])), sum8(sc * __uint_as_float(v0[3])));
    af[2] = pk2(sum8(sc * __uint_as_float(v1[0])), sum8(sc * __uint_as_float(v1[1])));
    af[3] = pk2(sum8(sc * __uint_as_float(v1[2])), sum8(sc * __uint_as_float(v1[3])));
    f32x4 d[4];
#pragma unroll
    for (int c = 0; c < 4; ++c) d[c] = mfma(af, bf[c], f32x4{0, 0, 0, 0});
    if (!(g & 1)) *(f32x4*)(lds + w * 128 + (g >> 1) * 64 + n * 4) = f32x4{d[0][0], d[1][0], d[2][0], d[3][0]};
    __syncthreads();
    if (tid < 32) {
      f32x4 s = {0, 0, 0, 0};
#pragma unroll
      for (int i = 0; i < 8; ++i) s += *(const f32x4*)(lds + i * 128 + tid * 4);
      __builtin_amdgcn_raw_buffer_store_b128(u32x4{nosent(__float_as_uint(s[0])), nosent(__float_as_uint(s[1])), nosent(__float_as_uint(s[2])), nosent(__float_as_uint(s[3]))},
                                             wsr, WS_A2 + wj * 8192 + wh * 512 + tid * 16, 0, 16);
    }
    STAMP(7);
  } else if (role == 2) {
    if (w < 7) {
      const int b = g & 1, dcol = 64 * w + 4 * n + 2 * (g >> 1);
      const unsigned xw = *(const GAS unsigned*)(p.x + b * DM + dcol);
      u64 mg[6];
#pragma nounroll
      for (int it = 0; it < SPIN; ++it) {
        bool ok = true;
#pragma unroll
        for (int i = 0; i < 6; ++i) { mg[i] = ldg(ws, WS_MODG + (xg * NMOD + (2 + (i >> 1)) * DM + dcol + (i & 1)) * 8); ok &= (unsigned)(mg[i] >> 32) == tagv; }
        if (__all(ok)) break;
      }
      float md[6];
#pragma unroll
      for (int i = 0; i < 6; ++i) md[i] = b ? bfhi((unsigned)mg[i]) : bflo((unsigned)mg[i]);
      md[4] = rbf(1.f + md[4]); md[5] = rbf(1.f + md[5]);
      u32x4 bf[2][2][4];
#pragma unroll
      for (int mt = 0; mt < 2; ++mt)
#pragma unroll
        for (int s = 0; s < 2; ++s)
#pragma unroll
          for (int i = 0; i < 4; ++i) {
            const int o = mt * 32 + s * 16 + 4 * i;
            bf[mt][s][0][i] = pair_lo(Wm[o], Wm[o + 2]); bf[mt][s][1][i] = pair_hi(Wm[o], Wm[o + 2]);
            bf[mt][s][2][i] = pair_lo(Wm[o + 1], Wm[o + 3]); bf[mt][s][3][i] = pair_hi(Wm[o + 1], Wm[o + 3]);
          }
#pragma unroll
      for (int i = 0; i < 16; ++i) KEEP(bf[i >> 3][(i >> 2) & 1][i & 3]);
#pragma unroll
      for (int i = 0; i < 6; ++i) KEEP(md[i]);
      u32x4 v[8];
#pragma unroll
      for (int i = 0; i < 8; ++i) v[i] = u32x4{SENT, SENT, SENT, SENT};
      {
        const unsigned char* pb = ws + WS_A2 + w * 8192 + 4096;
        unsigned pend = 0xff;
#pragma nounroll
        for (int it = 0; it < SPIN && pend; ++it) {
#define PLD(i) if (pend & (1u << i)) asm volatile("global_load_dwordx4 %0, %1, %2 offset:%3 sc1" : "+v"(v[i]) : "v"(voff), "s"(pb), "i"(i * 1024 - 4096) : "memory");
          PLD(0) PLD(1) PLD(2) PLD(3) PLD(4) PLD(5) PLD(6) PLD(7)
#undef PLD
          asm volatile("s_waitcnt vmcnt(0)" : "+v"(v[0]), "+v"(v[1]), "+v"(v[2]), "+v"(v[3]), "+v"(v[4]), "+v"(v[5]), "+v"(v[6]), "+v"(v[7]) :: "memory");
#pragma unroll
          for (int i = 0; i < 8; ++i) if (pend & (1u << i)) { if (__all(ok4(v[i]))) pend &= ~(1u << i); }
        }
      }
      STAMP(6);
      f32x4 s4 = __builtin_bit_cast(f32x4, v[0]);
#pragma unroll
      for (int i = 1; i < 8; ++i) s4 += __builtin_bit_cast(f32x4, v[i]);
      const float a0 = rbf(swap32_add(s4[0], s4[2])), a1 = rbf(swap32_add(s4[1], s4[3]));
      const unsigned x1p = pk2(bflo(xw) + rbf(md[0] * a0), bfhi(xw) + rbf(md[1] * a1));
      const float x0 = bflo(x1p), x1 = bfhi(x1p);
      if (slot == 0) __builtin_amdgcn_raw_buffer_store_b32(nosent(x1p), wsr, WS_X1 + (b * DM + dcol) * 2, 0, 16);
      float sm = sum16(x0 + x1); sm = swap32_add(sm, sm) * (1.f / 64);
      float sq = sum16((x0 - sm) * (x0 - sm) + (x1 - sm) * (x1 - sm)); sq = swap32_add(sq, sq);
      if (n == 0 && g < 2) *(f32x2*)(lds + 1536 + b * 16 + w * 2) = f32x2{sm, sq};
      __syncthreads();
      const f32x2 st = *(const f32x2*)(lds + 1536 + b * 16 + min(n & 7, 6) * 2);
      const bool live = (n & 7) < 7;
      const float mean = sum8(live ? st[0] : 0.f) * (1.f / 7);
      const float var = sum8(live ? st[1] + 64.f * (st[0] - mean) * (st[0] - mean) : 0.f) * (1.f / DM);
      const float rden = __builtin_amdgcn_rcpf(rbf(__builtin_sqrtf(rbf(rbf(var) + 1e-5f))));
      const float mb = rbf(mean);
      const unsigned hp = pk2(rbf(rbf(rbf(x0 - mb) * rden) * md[4]) + md[2], rbf(rbf(rbf(x1 - mb) * rden) * md[5]) + md[3]);
      unsigned* ht = (unsigned*)(lds + 1024 + w * 64);
      ht[b * 32 + 2 * n + (g >> 1)] = hp;
      FENCE();
      f32x4 d[2][4] = {};
#pragma unroll
      for (int s = 0; s < 2; ++s) {
        const u32x4 af = *(const u32x4*)(ht + (n & 1) * 32 + 16 * s + 4 * g);
#pragma unroll
        for (int mt = 0; mt < 2; ++mt)
#pragma unroll
          for (int c = 0; c < 4; ++c) d[mt][c] = mfma(af, bf[mt][s][c], d[mt][c]);
      }
      if (lane < 16) {
        float* r = lds + 4352 + w * 256 + n * 4;
#pragma unroll
        for (int mt = 0; mt < 2; ++mt)
#pragma unroll
          for (int bb = 0; bb < 2; ++bb) *(f32x4*)(r + mt * 128 + bb * 64) = f32x4{d[mt][0][bb], d[mt][1][bb], d[mt][2][bb], d[mt][3][bb]};
      }
    } else {
      __syncthreads();
    }
    __syncthreads();
    if (w == 0) {
      f32x2 gg = {0, 0}, uu = {0, 0};
#pragma unroll
      for (int i = 0; i < 7; ++i) { gg += *(const f32x2*)(lds + 4352 + i * 256 + lane * 2); uu += *(const f32x2*)(lds + 4352 + i * 256 + 128 + lane * 2); }
      const unsigned hh = pk2(rbf(sigm(rbf(gg[0]))) * rbf(uu[0]), rbf(sigm(rbf(gg[1]))) * rbf(uu[1]));
      __builtin_amdgcn_raw_buffer_store_b32(nosent(hh), wsr, WS_HH + (lane >> 5) * (FF * 2) + slot * 128 + (lane & 31) * 4, 0, 16);
    }
    STAMP(7);
  } else if (role == 3) {
    u32x4 bf[7];
#pragma unroll
    for (int s = 0; s < 7; ++s)
#pragma unroll
      for (int i = 0; i < 4; ++i) bf[s][i] = pair_lo(Wd[s * 8 + 2 * i], Wd[s * 8 + 2 * i + 1]);
#pragma unroll
    for (int i = 0; i < 7; ++i) KEEP(bf[i]);
    float gb = 0.f, x1 = 0.f;
    if (w == 0) {
      const int b = g & 1, col = 16 * slot + n;
      u64 gg;
#pragma nounroll
      for (int it = 0; it < SPIN; ++it) { gg = ldg(ws, WS_MODG + (xg * NMOD + 5 * DM + col) * 8); if (__all((unsigned)(gg >> 32) == tagv)) break; }
      gb = b ? bfhi((unsigned)gg) : bflo((unsigned)gg);
      unsigned xw;
#pragma nounroll
      for (int it = 0; it < SPIN; ++it) { FENCE(); xw = __builtin_amdgcn_raw_buffer_load_b32(wsr, WS_X1 + (b * DM + (col & ~1)) * 2, 0, 16); if (__all(xw != SENT)) break; }
      x1 = (col & 1) ? bfhi(xw) : bflo(xw);
    }
    u32x4 af = {0, 0, 0, 0};
    if (n < 14) {
      const unsigned o3 = WS_HH + (n & 1) * (FF * 2) + (224 * w + 32 * (n >> 1) + 8 * g) * 2;
#pragma nounroll
      for (int it = 0; it < SPIN; ++it) { FENCE(); af = __builtin_amdgcn_raw_buffer_load_b128(wsr, o3, 0, 16); if (ok4(af)) break; }
    }
    STAMP(6);
    f32x4 d[7];
#pragma unroll
    for (int s = 0; s < 7; ++s) d[s] = mfma(af, bf[s], f32x4{0, 0, 0, 0});
    f32x2 t0 = {d[0][0] + d[1][2], d[0][1] + d[1][3]}, t1 = {d[2][0] + d[3][2], d[2][1] + d[3][3]}, t2 = {d[4][0] + d[5][2], d[4][1] + d[5][3]};
    KEEP(t0); KEEP(t1); KEEP(t2);
    const bool g0 = g & 1, g1 = g & 2;
    const float y0 = g1 ? (g0 ? d[6][0] : t2[0]) : (g0 ? t1[0] : t0[0]);
    const float y1 = g1 ? (g0 ? d[6][1] : t2[1]) : (g0 ? t1[1] : t0[1]);
    float y = swap16_add(y0, y1); y = swap32_add(y, y);
    if (lane < 32) lds[w * 32 + lane] = y;
    __syncthreads();
    if (tid < 32) {
      float s = 0;
#pragma unroll
      for (int i = 0; i < 8; ++i) s += lds[i * 32 + tid];
      ((GAS u16*)p.out)[g * DM + 16 * slot + n] = (u16)pk2(x1 + rbf(gb * rbf(s)), 0.f);
      if (tid == 0 && slot == 0) __hip_atomic_store((GAS unsigned*)(ws + WS_NONCE), tagv, RLX_AGENT);
    }
    STAMP(7);
  }
}

__global__ void __launch_bounds__(512, 2) fused_block(Params p) {
  __shared__ __attribute__((aligned(16))) float lds[8192];
  const int tid = threadIdx.x, lane = tid & 63, w = __builtin_amdgcn_readfirstlane(tid >> 6);
  const int wg = blockIdx.x, xg = wg & 7, slot = wg >> 3;
  const int g = lane >> 4, n = lane & 15;
  unsigned char* const ws = p.ws;
  const rsrc_t wsr = mkrs(ws, WS_END);
  u64 ts[12] = {};
  STAMP(0);
  const unsigned nonce = __hip_atomic_load((const GAS unsigned*)(ws + WS_NONCE), RLX_AGENT);
#define GET_TAG() ({ unsigned t_; asm volatile("v_mov_b32 %0, %1" : "=v"(t_) : "v"(nonce)); (unsigned)__builtin_amdgcn_readfirstlane(t_) + 1u; })

  const int apair = ((slot - 8) >> 1) * 8 + xg;
  if (slot >= 8 && slot < 20 && apair < 42) {
    const int a = 2 * apair + (slot & 1);
    if (w < 7) {
      unsigned W[2][8]; u32x4 cv[2];
#pragma unroll
      for (int s = 0; s < 2; ++s) cv[s] = *(const GAS u32x4*)(p.c + (n & 1) * DM + 64 * w + 32 * s + 8 * g);
      const rsrc_t wr = mkrs(p.wm, DM * NMOD * 2);
#pragma unroll
      for (int s = 0; s < 2; ++s)
#pragma unroll
        for (int j = 0; j < 8; ++j) W[s][j] = __builtin_amdgcn_raw_buffer_load_b32(wr, (8 * g * NMOD + 2 * n) * 2, ((64 * w + 32 * s + j) * NMOD + 32 * a) * 2, 0);
      PIN();
      f32x4 d0 = {0, 0, 0, 0}, d1 = {0, 0, 0, 0};
#pragma unroll
      for (int s = 0; s < 2; ++s) {
        u32x4 af, b0, b1;
#pragma unroll
        for (int i = 0; i < 4; ++i) {
          const float lo = bflo(cv[s][i]), hi = bfhi(cv[s][i]);
          af[i] = pk2(lo * sigm(lo), hi * sigm(hi));
          b0[i] = pair_lo(W[s][2 * i], W[s][2 * i + 1]); b1[i] = pair_hi(W[s][2 * i], W[s][2 * i + 1]);
        }
        d0 = mfma(af, b0, d0); d1 = mfma(af, b1, d1);
      }
      if (lane < 16) *(f32x4*)(lds + w * 64 + n * 4) = f32x4{d0[0], d0[1], d1[0], d1[1]};
    } else if (a >= 28) {
#pragma unroll
      for (int i = 0; i < 6; ++i) __builtin_amdgcn_raw_buffer_store_b128(u32x4{SENT, SENT, SENT, SENT}, wsr, WS_CLR + (a - 28) * 6144 + i * 1024 + lane * 16, 0, 16);
      WAITVM(0);
    }
    __syncthreads();
    const unsigned tagv = GET_TAG();
    if (tid < 256) {
      const int cc = tid & 31, rep = tid >> 5;
      f32x2 s = {0, 0};
#pragma unroll
      for (int i = 0; i < 7; ++i) s += *(const f32x2*)(lds + i * 64 + cc * 2);
      stg(ws, WS_MODG + (rep * NMOD + 32 * a + cc) * 8, pk2(s[0], s[1]), tagv);
    }
    STAMP(1);
  }
  else if (slot < 8) {
    const int bq = slot * 8 + xg;
    if (w < 7) {
      u32x2 W[2][8]; u32x4 xa[2]; unsigned xo[2];
#pragma unroll
      for (int b = 0; b < 2; ++b) {
        xa[b] = __builtin_amdgcn_raw_buffer_load_b128(mkrs(p.x + b * DM, DM * 2), lane * 16, 0, 0);
        xo[b] = ((const GAS u16*)p.x)[b * DM + 64 * w + lane];
      }
      const rsrc_t wr = mkrs(p.wq, DM * HD * 2);
#pragma unroll
      for (int s = 0; s < 2; ++s)
#pragma unroll
        for (int j = 0; j < 8; ++j) W[s][j] = __builtin_amdgcn_raw_buffer_load_b64(wr, (8 * g * HD + 4 * n) * 2, ((64 * w + 32 * s + j) * HD + 64 * bq) * 2, NT);
      PIN();
      float nrm[2];
#pragma unroll
      for (int b = 0; b < 2; ++b) {
        float s = 0;
#pragma unroll
        for (int i = 0; i < 4; ++i) s += bflo(xa[b][i]) + bfhi(xa[b][i]);
        const float mean = sum64(s) * (1.f / DM);
        float v = 0;
#pragma unroll
        for (int i = 0; i < 4; ++i) { const float lo = bflo(xa[b][i]) - mean, hi = bfhi(xa[b][i]) - mean; v += lo * lo + hi * hi; }
        const float den = rbf(__builtin_sqrtf(rbf(rbf(sum64(lane < 56 ? v : 0.f) * (1.f / DM)) + 1e-5f)));
        nrm[b] = rbf(rbf(bflo(xo[b]) - rbf(mean)) / den);
      }
      u32x4 bf[2][4];
#pragma unroll
      for (int s = 0; s < 2; ++s)
#pragma unroll
        for (int i = 0; i < 4; ++i) {
          bf[s][0][i] = pair_lo(W[s][2 * i][0], W[s][2 * i + 1][0]); bf[s][1][i] = pair_hi(W[s][2 * i][0], W[s][2 * i + 1][0]);
          bf[s][2][i] = pair_lo(W[s][2 * i][1], W[s][2 * i + 1][1]); bf[s][3][i] = pair_hi(W[s][2 * i][1], W[s][2 * i + 1][1]);
        }
      const unsigned tagv = GET_TAG();
      KEEP(nrm[0]); KEEP(nrm[1]);
#pragma unroll
      for (int i = 0; i < 8; ++i) KEEP(bf[i >> 2][i & 3]);
      PIN();
      u64 gs, gc;
#pragma nounroll
      for (int it = 0; it < SPIN; ++it) {
        gs = ldg(ws, WS_MODG + (xg * NMOD + 64 * w + lane) * 8);
        gc = ldg(ws, WS_MODG + (xg * NMOD + DM + 64 * w + lane) * 8);
        if (__all((unsigned)(gs >> 32) == tagv && (unsigned)(gc >> 32) == tagv)) break;
      }
      STAMP(1);
      const float h0 = rbf(nrm[0] * rbf(1.f + bflo((unsigned)gc))) + bflo((unsigned)gs);
      const float h1 = rbf(nrm[1] * rbf(1.f + bfhi((unsigned)gc))) + bfhi((unsigned)gs);
      const unsigned hp = pk2(h0, h1);
      u16* ht = (u16*)(lds + 1024 + w * 64);
      ht[lane] = (u16)hp; ht[64 + lane] = (u16)(hp >> 16);
      FENCE();
      f32x4 d[4] = {};
#pragma unroll
      for (int s = 0; s < 2; ++s) {
        const u32x4 af = *(const u32x4*)(ht + (n & 1) * 64 + 32 * s + 8 * g);
#pragma unroll
        for (int c = 0; c < 4; ++c) d[c] = mfma(af, bf[s][c], d[c]);
      }
      if (lane < 16) {
        *(f32x4*)(lds + w * 128 + n * 4) = f32x4{d[0][0], d[1][0], d[2][0], d[3][0]};
        *(f32x4*)(lds + w * 128 + 64 + n * 4) = f32x4{d[0][1], d[1][1], d[2][1], d[3][1]};
      }
    }
    __syncthreads();
    {
      const unsigned tagv = GET_TAG();
      f32x2 s = {0, 0};
#pragma unroll
      for (int i = 0; i < 7; ++i) s += *(const f32x2*)(lds + i * 128 + lane * 2);
      stg(ws, WS_QG + ((((lane >> 5) * NH + (bq >> 2)) * 8 + w) * 128 + (bq & 3) * 32 + (lane & 31)) * 8, pk2(s[0], s[1]), tagv);
    }
    STAMP(2);
  }

  const rsrc_t kr = mkrs(p.kc + (size_t)slot * (SK * DH), SK * DH * 2), vr = mkrs(p.vc + (size_t)slot * (SK * DH), SK * DH * 2);
  const unsigned voff = lane * 16;
  const unsigned u0 = (unsigned)(xg * (NPRE * 8) + w) * 4096u;
  Unit ua, ub, uc, ud;
  PIN();
  ld4nt(ua.k, kr, u0, voff); ld4nt(ua.v, vr, u0, voff);
  WAITVM(4); ld4nt(ub.k, kr, u0 + 32768, voff);
  WAITVM(4); ld4nt(ub.v, vr, u0 + 32768, voff);
  WAITVM(4); ld4nt(uc.k, kr, u0 + 65536, voff);
  WAITVM(4); ld4nt(uc.v, vr, u0 + 65536, voff);
  WAITVM(4);
  const unsigned tagv = GET_TAG();
  u64 qg[5];
  const unsigned qoff = WS_QG + ((slot * 8 + xg) * 128 + (lane & 31) * 4) * 8;
  const unsigned foff = WS_MODG + (xg * NMOD + 32 * (28 + min(lane, 55))) * 8;
#pragma unroll
  for (int i = 0; i < 4; ++i) qg[i] = ldg(ws, qoff + i * 8);
  qg[4] = ldg(ws, foff);
  FENCE();
  ld4nt(ud.k, kr, u0 + 98304, voff);
  WAITVM(9); ld4nt(ud.v, vr, u0 + 98304, voff);
  PIN();
#define TG(v) ((unsigned)((v) >> 32) ^ tagv)
#define Q_OK() __all((TG(qg[0]) | TG(qg[1]) | TG(qg[2]) | TG(qg[3]) | TG(qg[4])) == 0)
  if (!Q_OK()) {
#pragma nounroll
    for (int it = 0; it < SPIN; ++it) {
#pragma unroll
      for (int i = 0; i < 4; ++i) qg[i] = ldg(ws, qoff + i * 8);
      qg[4] = ldg(ws, foff);
      if (Q_OK()) break;
    }
  }
  STAMP(3);
  const u32x4 q = {(unsigned)qg[0], (unsigned)qg[1], (unsigned)qg[2], (unsigned)qg[3]};
  unsigned lz = lane;
  asm volatile("" : "+v"(lz));
  GAS unsigned* const cnt = (GAS unsigned*)(ws + WS_CNT + slot * 256) + lz;
  unsigned tk = 0;
  if (lane == 0) tk = __hip_atomic_fetch_add(cnt, 1u, RLX_AGENT);
  const bool b3 = lane & 8;
  Att at; at.m = -INFINITY; at.l = 0.f;
#pragma unroll
  for (int k = 0; k < 4; ++k) at.acc[k] = f32x2{0.f, 0.f};
  attend<true>(ua, q, at, b3, kr, vr, u0 + 131072, voff);
  attend<false>(ub, q, at, b3, kr, vr, 0, 0);
  attend<false>(uc, q, at, b3, kr, vr, 0, 0);
  attend<false>(ud, q, at, b3, kr, vr, 0, 0);

  const int wh = (xg - 2) + 6 * (slot / 7), wj = slot % 7;
  const int role = (xg == 0 && slot < 28) ? 2 : (xg == 1 && slot < 28) ? 3 : (xg >= 2 && slot < 21 && wh < 16) ? 1 : 0;
#define REST(r) stream_and_tail(r, p, lds, ua, q, at, tk, cnt, kr, vr, wsr, tagv, wh, wj, ts)
  REST(role);
  STAMP(8);
  if (ts[PROBE]) { while (__builtin_amdgcn_s_memrealtime() < ts[PROBE] + 7000) __builtin_amdgcn_s_sleep(1); }
}

extern "C" void kernel_launch(void* const* d_in, const int* in_sizes, int n_in, void* d_out, int out_size, void* d_ws, size_t ws_size, hipStream_t stream) {
  Params p;
  p.x = (const u16*)d_in[0]; p.kc = (const u16*)d_in[1]; p.vc = (const u16*)d_in[2]; p.wq = (const u16*)d_in[3]; p.wo = (const u16*)d_in[4];
  p.wg = (const u16*)d_in[6]; p.wu = (const u16*)d_in[7]; p.wd = (const u16*)d_in[8]; p.c = (const u16*)d_in[9]; p.wm = (const u16*)d_in[10];
  p.out = (u16*)d_out; p.ws = (unsigned char*)d_ws;
  fused_block<<<256, 512, 0, stream>>>(p);
}
```

```cpp
#define LV3 8
#define CP0 0
#include <hip/hip_runtime.h>
#include <stdint.h>

typedef unsigned short u16;
typedef unsigned long long u64;
typedef unsigned u32x4 __attribute__((ext_vector_type(4)));
typedef unsigned u32x3 __attribute__((ext_vector_type(3)));
typedef unsigned u32x2 __attribute__((ext_vector_type(2)));
typedef int i32x4 __attribute__((ext_vector_type(4)));
typedef short s16x4 __attribute__((ext_vector_type(4)));
typedef float f32x4 __attribute__((ext_vector_type(4)));
typedef float f32x2 __attribute__((ext_vector_type(2)));
typedef __bf16 bf16x2 __attribute__((ext_vector_type(2)));
typedef __bf16 bf16x8 __attribute__((ext_vector_type(8)));
typedef __amdgpu_buffer_rsrc_t rsrc_t;
#define GAS __attribute__((address_space(1)))
#define LAS __attribute__((address_space(3)))
#define RLX_AGENT __ATOMIC_RELAXED, __HIP_MEMORY_SCOPE_AGENT
#define FENCE() asm volatile("" ::: "memory")
#define SCHED() __builtin_amdgcn_sched_barrier(0)
#define PIN() do { FENCE(); SCHED(); } while (0)
#define KEEP(x) asm volatile("" : "+v"(x))
#define WAITVM(n) asm volatile("s_waitcnt vmcnt(" #n ")" ::: "memory")
#define LGKM0() asm volatile("s_waitcnt lgkmcnt(0)" ::: "memory")
#define BARRIER() do { asm volatile("s_waitcnt lgkmcnt(0)\n\ts_barrier" ::: "memory"); SCHED(); } while (0)
#define LIKELY(c) __builtin_expect(!!(c), 1)
#define UNLIKELY(c) __builtin_expect(!!(c), 0)

constexpr int DM = 448, NH = 16, SK = 8192, DH = 256, FF = 1792, NMOD = 6 * DM, HD = NH * DH;
constexpr int UPH = SK / 8;
#define SLP 64
#define FLS 4352
#define ME 4
#define DE 2
#define LV0 4
#define LV1 5
#define LV2 6
#define SCH0 600
#define SCH1 1075
#define SCHC 118
#define CPS 2
#define OFW 50
#define OFM 50
#define OFD 0
#define OFFS(rid) ((rid) >= R_W && (rid) < R_M ? -OFW : (rid) >= R_M && (rid) < R_D ? -OFM : (rid) >= R_D && (rid) < R_NONE ? -OFD : 0)
#define SCHW 6
#define NEND 8
#define GROW 8
#define NIF 5
#define CODEB 32700
#define WARM 1
#define REPM 1
#define REPQ 1
#define NCAL 4
#define QROW 10
#define NPOLL 1
#define BDEL 530
#define OUTF ""
#define CFS 8
#define HEAD 1
#define NEAR 1
constexpr int XM = 1, XD = 3;
constexpr int SPIN = 1 << 20;
constexpr unsigned SENT = 0xFFFFFFFFu;
constexpr unsigned OOB = 0x7ffffff0u;
constexpr float CEXP = 0.0625f * 1.4426950408889634f;
constexpr int NT = 2, SC1 = 16;

constexpr unsigned WS_NONCE = 0;
constexpr unsigned WS_CFG = 1024;
constexpr unsigned WS_T0 = 4096;
constexpr unsigned WS_CP = 4608;
constexpr unsigned WS_CAL = 16384;
constexpr unsigned WS_A1 = 65536;
constexpr unsigned WS_A2 = WS_A1 + 8 * 81920;
constexpr unsigned WS_HH = WS_A2 + 65536;
constexpr unsigned WS_X1 = WS_HH + 16384;
constexpr unsigned WS_MODG = WS_X1 + 16384;
constexpr unsigned WS_QG = WS_MODG + REPM * NMOD * 8;
constexpr unsigned WS_FL = WS_QG + 32 * REPQ * 1024;
constexpr unsigned WS_END = WS_FL + 32 * FLS;
__device__ __forceinline__ unsigned blk(const unsigned off) { return (off >> 13 << 14) | (off & 8191u); }
__device__ __forceinline__ unsigned place(const unsigned cfg, const int grp) { return ((cfg >> grp) & 1u) << 13; }
constexpr int L_RED = 0, L_HT = 1024, L_ST = 1536, L_Q = 1664, L_MRG = 1792, L_CTL = 2848, L_Z = 3072, L_FIN = 4096, L_BIG = 20480;
constexpr int C_Q = 0, C_CLR = 1, C_A = 2, C_MRG = 3, C_LAND = 4, C_DONE = 8, C_TAG = 12, C_PREP = 13, C_FIN = 14, C_KEND = 15, C_VMID = 16, C_MAX = 17, C_RL = 18, C_TAGOK = 19, C_QGO = 20, C_T0 = 21, C_MARK = 32;
static_assert(GROW % 4 == 0 && GROW <= 20, "");
static_assert(NIF >= 2 && NIF <= 8, "a quad set is complete once eight younger loads have been issued");

#define TL() do { } while (0)
#define STAMP(i) do { } while (0)

struct Params { const u16 *x, *kc, *vc, *wq, *wo, *wg, *wu, *wd, *c, *wm; u16* out; unsigned char* ws; unsigned role[64]; };
struct Ptrs { const u16 *x, *kc, *vc, *wq, *wo, *wg, *wu, *wd, *c, *wm; u16* out; unsigned char* ws; };
constexpr int R_A = 0, R_B = 42, R_W = 106, R_M = 162, R_D = R_M + 56, R_NONE = R_D + 28;

__device__ __forceinline__ float bflo(unsigned v) { return __uint_as_float(v << 16); }
__device__ __forceinline__ float bfhi(unsigned v) { return __uint_as_float(v & 0xffff0000u); }
__device__ __forceinline__ unsigned pk2(float lo, float hi) { bf16x2 r = {(__bf16)lo, (__bf16)hi}; return __builtin_bit_cast(unsigned, r); }
__device__ __forceinline__ float rbf(float f) { return (float)(__bf16)f; }
__device__ __forceinline__ f32x2 rbf2(f32x2 v) { unsigned u = pk2(v[0], v[1]); KEEP(u); return f32x2{bflo(u), bfhi(u)}; }
__device__ __forceinline__ float dot2(unsigned a, unsigned b, float acc) {
  return __builtin_amdgcn_fdot2_f32_bf16(__builtin_bit_cast(bf16x2, a), __builtin_bit_cast(bf16x2, b), acc, false);
}
template <int CTRL> __device__ __forceinline__ float dpp(float x) {
  return __builtin_bit_cast(float, __builtin_amdgcn_mov_dpp(__builtin_bit_cast(int, x), CTRL, 0xf, 0xf, true));
}
constexpr int XOR1 = 0xB1, XOR2 = 0x4E, HMIR = 0x141, ROR4 = 0x124, ROR8 = 0x128, BC0 = 0x150, BC8 = 0x158;
__device__ __forceinline__ float fmx(float a, float b) { return __builtin_elementwise_maximum(a, b); }
__device__ __forceinline__ f32x2 swap16(float a, float b) {
  auto s = __builtin_amdgcn_permlane16_swap(__float_as_uint(a), __float_as_uint(b), false, false);
  return f32x2{__uint_as_float(s[0]), __uint_as_float(s[1])};
}
__device__ __forceinline__ f32x2 swap32(float a, float b) {
  auto s = __builtin_amdgcn_permlane32_swap(__float_as_uint(a), __float_as_uint(b), false, false);
  return f32x2{__uint_as_float(s[0]), __uint_as_float(s[1])};
}
__device__ __forceinline__ float swap16_add(float a, float b) { const f32x2 s = swap16(a, b); return s[0] + s[1]; }
__device__ __forceinline__ float swap32_add(float a, float b) { const f32x2 s = swap32(a, b); return s[0] + s[1]; }
__device__ __forceinline__ float sum8(float v) { v += dpp<XOR1>(v); v += dpp<XOR2>(v); v += dpp<HMIR>(v); return v; }
__device__ __forceinline__ float sum16(float v) { v = sum8(v); v += dpp<ROR8>(v); return v; }
__device__ __forceinline__ float sum_groups(float v) { v += dpp<ROR8>(v); v = swap16_add(v, v); return swap32_add(v, v); }
__device__ __forceinline__ float sum64(float v) { return sum_groups(sum8(v)); }
__device__ __forceinline__ float max_groups(float z) {
  z = fmx(z, dpp<ROR8>(z));
  f32x2 s = swap16(z, z); z = fmx(s[0], s[1]);
  s = swap32(z, z); return fmx(s[0], s[1]);
}
__device__ __forceinline__ float sigm(float v) { return __builtin_amdgcn_rcpf(1.f + __builtin_amdgcn_exp2f(-1.4426950408889634f * v)); }
__device__ __forceinline__ f32x4 mfma(u32x4 a, u32x4 b, f32x4 c) {
  return __builtin_amdgcn_mfma_f32_16x16x32_bf16(__builtin_bit_cast(bf16x8, a), __builtin_bit_cast(bf16x8, b), c, 0, 0, 0);
}
__device__ __forceinline__ unsigned pair_lo(unsigned r0, unsigned r1) { return __builtin_amdgcn_perm(r1, r0, 0x05040100u); }
__device__ __forceinline__ unsigned pair_hi(unsigned r0, unsigned r1) { return __builtin_amdgcn_perm(r1, r0, 0x07060302u); }
__device__ __forceinline__ unsigned nosent(unsigned v) { return min(v, SENT - 1u); }
__device__ __forceinline__ unsigned nosent(float v) { return min(__float_as_uint(v), SENT - 1u); }
__device__ __forceinline__ unsigned mx3(unsigned a, unsigned b, unsigned c) { return max(max(a, b), c); }
__device__ __forceinline__ unsigned x3(unsigned a, unsigned b, unsigned c) { return __builtin_amdgcn_bitop3_b32(a, b, c, 0x96); }
__device__ __forceinline__ u64 quads(const u64 m) { u64 r; asm("s_wqm_b64 %0, %1" : "=s"(r) : "s"(m) : "scc"); return r; }
__device__ __forceinline__ unsigned mx4(u32x4 v) { return max(mx3(v[0], v[1], v[2]), v[3]); }
__device__ __forceinline__ u64 ldg(const unsigned char* ws, unsigned off) { return __hip_atomic_load((const GAS u64*)(ws + off), RLX_AGENT); }
__device__ __forceinline__ void stg(unsigned char* ws, unsigned off, unsigned v, unsigned tag) {
  __hip_atomic_store((GAS u64*)(ws + off), ((u64)tag << 32) | v, RLX_AGENT);
}
__device__ __forceinline__ rsrc_t mkrs(const void* base, unsigned bytes) { return __builtin_amdgcn_make_buffer_rsrc((void*)base, 0, bytes, 0x00020000); }
__device__ __forceinline__ i32x4 mksrd(const void* base, unsigned bytes) { const u64 a = (u64)base; return i32x4{(int)(unsigned)a, (int)((unsigned)(a >> 32) & 0xffffu), (int)bytes, 0x00020000}; }

__device__ __forceinline__ void pub(const u32x4 v, const i32x4 r, const unsigned o) { asm volatile("buffer_store_dwordx4 %0, %1, %2, 0 offen sc1\n\ts_nop 1" :: "v"(v), "v"(o), "s"(r) : "memory"); }
__device__ __forceinline__ void pub(const u32x3 v, const i32x4 r, const unsigned o) { asm volatile("buffer_store_dwordx3 %0, %1, %2, 0 offen sc1\n\ts_nop 1" :: "v"(v), "v"(o), "s"(r) : "memory"); }
__device__ __forceinline__ void pub(const u32x2 v, const i32x4 r, const unsigned o) { asm volatile("buffer_store_dwordx2 %0, %1, %2, 0 offen sc1" :: "v"(v), "v"(o), "s"(r) : "memory"); }
__device__ __forceinline__ void pub(const unsigned v, const i32x4 r, const unsigned o) { asm volatile("buffer_store_dword %0, %1, %2, 0 offen sc1" :: "v"(v), "v"(o), "s"(r) : "memory"); }
#define GO(left) ({ u64 g_; asm volatile("s_sub_u32 %1, %1, 1\n\ts_cselect_b64 %0, 0, -1" : "=s"(g_), "+s"(left) :: "scc"); g_; })
#define KEEPS(x) asm volatile("" : "+s"(x))
#define LDSP(T, a) ((LAS T*)(size_t)(a))

#define HOP(a, b) asm volatile("hop_" #a ":\n\ts_cbranch_execz hop_" #b)
#define HOP_RET() asm volatile("s_branch 1f\nhop_ret:\n\ts_setpc_b64 s[76:77]\n1:")
#define HOP_CALL(a) asm volatile("s_mov_b64 exec, 0\n\ts_call_b64 s[76:77], hop_" #a "\n\ts_mov_b64 exec, -1" ::: "s76", "s77")

template <int N = NIF>
__device__ __forceinline__ void ld4(u32x4 (&d)[4], const i32x4 r, const unsigned so, const unsigned voff) {
  asm volatile("s_waitcnt vmcnt(%7)\n\tbuffer_load_dwordx4 %0, %4, %5, %6 offen nt\n\t"
               "s_waitcnt vmcnt(%7)\n\tbuffer_load_dwordx4 %1, %4, %5, %6 offen offset:1024 nt\n\t"
               "s_waitcnt vmcnt(%7)\n\tbuffer_load_dwordx4 %2, %4, %5, %6 offen offset:2048 nt\n\t"
               "s_waitcnt vmcnt(%7)\n\tbuffer_load_dwordx4 %3, %4, %5, %6 offen offset:3072 nt"
               : "+v"(d[0]), "+v"(d[1]), "+v"(d[2]), "+v"(d[3]) : "v"(voff), "s"(r), "s"(so), "i"(N - 1) : "memory");
}
#define SEQ(n) "s_waitcnt vmcnt(" n ")\n\tbuffer_load_dwordx4 %0, %4, %5, %6 offen nt\n\t" \
               "s_waitcnt vmcnt(" n ")\n\tbuffer_load_dwordx4 %1, %4, %5, %6 offen offset:1024 nt\n\t" \
               "s_waitcnt vmcnt(" n ")\n\tbuffer_load_dwordx4 %2, %4, %5, %6 offen offset:2048 nt\n\t" \
               "s_waitcnt vmcnt(" n ")\n\tbuffer_load_dwordx4 %3, %4, %5, %6 offen offset:3072 nt\n\t"
__device__ __forceinline__ void ld4b(u32x4 (&d)[4], const i32x4 r, const unsigned so, const unsigned voff, const unsigned jump) {
  asm volatile("s_getpc_b64 vcc\n\ts_add_u32 vcc_lo, vcc_lo, %7\n\ts_addc_u32 vcc_hi, vcc_hi, 0\n\ts_setpc_b64 vcc\n\t"
               SEQ("%8") "s_branch 1f\n\t" SEQ("%9") "s_branch 1f\n\t" SEQ("%10") "s_branch 1f\n\t" SEQ("%11") "1:"
               : "+v"(d[0]), "+v"(d[1]), "+v"(d[2]), "+v"(d[3]) : "v"(voff), "s"(r), "s"(so), "s"(jump), "i"(LV0 - 1), "i"(LV1 - 1), "i"(LV2 - 1), "i"(LV3 - 1) : "memory", "scc", "vcc");
}
template <int N = NIF>
__device__ __forceinline__ void dma4(const unsigned ldsaddr, const i32x4 r, const unsigned so, const unsigned voff) {
  unsigned keep;
  asm volatile("s_mov_b32 %0, m0\n\ts_mov_b32 m0, %1\n\t"
               "s_waitcnt vmcnt(%5)\n\tbuffer_load_dwordx4 %2, %3, %4 offen nt lds\n\t"
               "s_waitcnt vmcnt(%5)\n\tbuffer_load_dwordx4 %2, %3, %4 offen offset:1024 nt lds\n\t"
               "s_waitcnt vmcnt(%5)\n\tbuffer_load_dwordx4 %2, %3, %4 offen offset:2048 nt lds\n\t"
               "s_waitcnt vmcnt(%5)\n\tbuffer_load_dwordx4 %2, %3, %4 offen offset:3072 nt lds\n\t"
               "s_mov_b32 m0, %0"
               : "=&s"(keep) : "s"(ldsaddr), "v"(voff), "s"(r), "s"(so), "i"(N - 1) : "memory");
}
__device__ __forceinline__ void dma1(const unsigned ldsaddr, const i32x4 r, const unsigned so, const unsigned vo) {
  unsigned keep;
  asm volatile("s_mov_b32 %0, m0\n\ts_mov_b32 m0, %1\n\ts_nop 0\n\tbuffer_load_dwordx4 %2, %3, %4 offen lds\n\ts_mov_b32 m0, %0"
               : "=&s"(keep) : "s"(ldsaddr), "v"(vo), "s"(r), "s"(so) : "memory");
}
#define LANDED(d) asm volatile("" : "+v"(d[0]), "+v"(d[1]), "+v"(d[2]), "+v"(d[3]) :: "memory")

struct Unit { u32x4 k[4], v[4]; };
struct Att { f32x2 acc[4]; };

__device__ __forceinline__ float qk(const u32x4 (&k)[4], const u32x4 q, const bool b3) {
  float s[4];
#pragma unroll
  for (int t = 0; t < 4; ++t) {
    float d = dot2(k[t][0], q[0], 0.f);
    d = dot2(k[t][1], q[1], d); d = dot2(k[t][2], q[2], d); s[t] = dot2(k[t][3], q[3], d);
  }
  const float s01 = swap16_add(s[0], s[1]), s23 = swap16_add(s[2], s[3]);
  return rbf(sum8((b3 ? s23 : s01) + dpp<ROR8>(b3 ? s01 : s23)));
}
__device__ __forceinline__ void pv1(const u32x4 v, const float p, Att& a) {
#pragma unroll
  for (int i = 0; i < 4; ++i) a.acc[i] = __builtin_elementwise_fma(f32x2{bflo(v[i]), bfhi(v[i])}, f32x2{p, p}, a.acc[i]);
}
__device__ __forceinline__ void pv(const u32x4 (&v)[4], const f32x4 p, Att& a) {
#pragma unroll
  for (int t = 0; t < 4; ++t) pv1(v[t], p[t], a);
  asm volatile("" : "+v"(a.acc[0]), "+v"(a.acc[1]), "+v"(a.acc[2]), "+v"(a.acc[3]) :: "memory");
}

struct Tile { u32x2 w[2][8]; };
__device__ __forceinline__ void tile_load(Tile& t, const rsrc_t r, const int ld, const unsigned soff, const int g, const int n) {
#pragma unroll
  for (int s = 0; s < 2; ++s)
#pragma unroll
    for (int j = 0; j < 8; ++j) t.w[s][j] = __builtin_amdgcn_raw_buffer_load_b64(r, (8 * g * ld + 4 * n) * 2, soff + (32 * s + j) * ld * 2, NT);
}
__device__ __forceinline__ void tile_frags(const Tile& t, u32x4 (&bf)[2][4]) {
#pragma unroll
  for (int s = 0; s < 2; ++s)
#pragma unroll
    for (int i = 0; i < 4; ++i) {
      bf[s][0][i] = pair_lo(t.w[s][2 * i][0], t.w[s][2 * i + 1][0]); bf[s][1][i] = pair_hi(t.w[s][2 * i][0], t.w[s][2 * i + 1][0]);
      bf[s][2][i] = pair_lo(t.w[s][2 * i][1], t.w[s][2 * i + 1][1]); bf[s][3][i] = pair_hi(t.w[s][2 * i][1], t.w[s][2 * i + 1][1]);
    }
}
__device__ __forceinline__ u32x4 tr_frag(const unsigned char* a, const int step) {
  const u32x2 lo = __builtin_bit_cast(u32x2, __builtin_amdgcn_ds_read_tr16_b64_v4i16((LAS s16x4*)a));
  const u32x2 hi = __builtin_bit_cast(u32x2, __builtin_amdgcn_ds_read_tr16_b64_v4i16((LAS s16x4*)(a + step)));
  return u32x4{lo[0], lo[1], hi[0], hi[1]};
}
__device__ __forceinline__ unsigned arrive_issue(const unsigned addr, const unsigned one) {
  unsigned r;
  asm volatile("s_mov_b64 exec, 1\n\tds_add_rtn_u32 %0, %1, %2\n\ts_mov_b64 exec, -1" : "=&v"(r) : "v"(addr), "v"(one) : "memory");
  return r;
}
#define ARRIVED(r) ({ asm volatile("s_waitcnt lgkmcnt(0)" : "+v"(r) :: "memory"); (unsigned)__builtin_amdgcn_readfirstlane(r); })

__global__ void __launch_bounds__(768, 3) fused_block(Params prm) {
  __shared__ __attribute__((aligned(16))) unsigned char smem[L_BIG + 131072];
  Ptrs p = {prm.x, prm.kc, prm.vc, prm.wq, prm.wo, prm.wg, prm.wu, prm.wd, prm.c, prm.wm, prm.out, prm.ws};
  unsigned rword = prm.role[blockIdx.x >> 2];
  unsigned junk;
  asm volatile("v_cmp_gt_u32 vcc, 64, %1\n\ts_cbranch_vccz 1f\n\ts_getpc_b64 s[72:73]\n\ts_and_b32 s73, s73, 0xffff\n\ts_mov_b32 s74, %2\n\ts_mov_b32 s75, 0x20000\n\t"
               "v_lshlrev_b32 %0, 10, %1\n\tbuffer_load_dword %0, %0, s[72:75], %3 offen\n1:"
               : "=&v"(junk) : "v"(threadIdx.x), "i"(CODEB), "s"((blockIdx.x >> 3) % 8 * 128) : "s72", "s73", "s74", "s75", "scc", "vcc");
  SCHED();
  const unsigned lds0 = (unsigned)(size_t)(LAS unsigned char*)smem;
  const int tid = threadIdx.x, lane = tid & 63, wv = __builtin_amdgcn_readfirstlane(tid >> 6);
  const int wg = blockIdx.x, xg = wg & 7, slot = wg >> 3;
  const unsigned voff = lane * 16;
  const i32x4 kr = mksrd(p.kc + (size_t)slot * (SK * DH), SK * DH * 2);
  const unsigned u0 = xg * 4 + wv, lbase = lds0 + L_BIG + wv * 32768;
#define ROW(i) ((((i) >> 1) * 32 + ((i) & 1) * 512 + u0) * 4096u)
  if (HEAD && wv < 4) dma4<8>(lbase, kr, ROW(0), voff);
  SCHED();
  asm volatile("" : "+s"(p.x), "+s"(p.vc), "+s"(p.wq), "+s"(p.wo), "+s"(p.wg), "+s"(p.wu), "+s"(p.wd), "+s"(p.c), "+s"(p.wm), "+s"(p.out), "+s"(p.ws), "+s"(rword));
  float* const lds = (float*)smem;
  volatile LAS unsigned* const ctl = (volatile LAS unsigned*)smem + L_CTL;
  const int g = lane >> 4, n = lane & 15;
  unsigned char* const ws = p.ws;
  const rsrc_t wsr = mkrs(ws, WS_END);
  const int rid = (rword >> (8 * (wg & 3))) & 255;
  const int zoff = (g >> 1) * 4 + ((lane >> 3) & 1) * 2 + (g & 1);

  if (wv < 4) {
    const i32x4 vr = mksrd(p.vc + (size_t)slot * (SK * DH), SK * DH * 2);
    if (tid < 128) ctl[tid] = 0;
    HOP_RET();
    STAMP(6);
    BARRIER();
    STAMP(7);
    Unit ua, ub, uc;
#pragma unroll
    for (int i = 0; i < 4; ++i) { asm volatile("" : "=v"(ua.k[i]), "=v"(ua.v[i]), "=v"(ub.k[i]), "=v"(ub.v[i]), "=v"(uc.k[i]), "=v"(uc.v[i])); }
#pragma unroll
    for (int i = HEAD; i < 8; ++i) { dma4(lbase + i * 4096, kr, ROW(i), voff); if (i > 2 && (i & 1)) TL(); }
    asm volatile("" :: "v"(junk));
    HOP(s0, s1);
    ld4(uc.k, kr, ROW(8), voff); ld4(uc.v, kr, ROW(9), voff); TL();
    ctl[C_LAND + wv] = 1;
    if (QROW == 9) ctl[C_QGO] = 1;
    ld4(ua.k, kr, ROW(10), voff); if (QROW == 10) ctl[C_QGO] = 1; ld4(ua.v, kr, ROW(11), voff); TL();
    if (QROW == 11) ctl[C_QGO] = 1;
    ld4(ub.k, kr, ROW(12), voff); ld4(ub.v, kr, ROW(13), voff); TL();
    STAMP(1);
    while (ctl[C_Q] == 0) __builtin_amdgcn_s_sleep(1);
    FENCE();
    STAMP(3);
    const u32x4 q = *(const u32x4*)(lds + L_Q + (lane & 31) * 4);
    unsigned run = (OFFS(rid) - SCHC) << 4, mka = lds0 + (L_CTL + C_MARK + wv) * 4;
    KEEP(mka);
#define LEVEL(mark) ({ const unsigned now_ = (unsigned)__builtin_amdgcn_s_memrealtime(), t0_ = __builtin_amdgcn_readfirstlane(ctl[C_T0]); unsigned j_; \
      asm volatile("s_lshl_b32 %0, %2, 4\n\ts_sub_i32 %0, %0, %1\n\ts_sub_i32 %0, %0, %5\n\ts_ashr_i32 %0, %0, %3\n\ts_max_i32 %0, %0, 0\n\ts_min_i32 %0, %0, 3\n\t" \
      "s_mul_i32 %0, %0, 52\n\ts_add_i32 %0, %0, 12\n\ts_add_i32 %1, %1, %4" : "=&s"(j_), "+s"(run) : "s"(now_), "i"(SCHW + 4), "i"(SCH1 * 16 / 10), "s"(t0_) : "scc"); \
      if (mark) { *(volatile LAS unsigned*)(size_t)mka = now_ | 1u; mka += 16; } j_; })
    const bool b3 = lane & 8;
    float* const zs = lds + L_Z + wv * 256;
#define KROW(X, row) { LANDED(X); const float z_ = qk(X, q, b3); if (!(lane & 7)) zs[(row) * 8 + zoff] = z_; FENCE(); }
    HOP(s1, s2);
    KROW(ua.k, 10) KROW(ua.v, 11)
#pragma nounroll
    for (int r = 12; r < 32; r += 4) {
      const bool last = r == 28;
      unsigned fast = LEVEL(1);
      const i32x4 nr = last ? vr : kr;
      const int n0 = last ? 0 : r + 4;
      HOP(s2, s3);
      ld4b(ua.k, kr, ROW(r + 2), voff, fast); KROW(ub.k, r)
      ld4b(ua.v, kr, ROW(r + 3), voff, fast); KROW(ub.v, r + 1) TL();
      if (r == 12) { HOP(s3, s4); KROW(uc.k, 8) KROW(uc.v, 9) }
      fast = LEVEL(0);
      HOP(s4, s5);
      ld4b(ub.k, nr, ROW(n0), voff, fast); KROW(ua.k, r + 2)
      ld4b(ub.v, nr, ROW(n0 + 1), voff, fast); KROW(ua.v, r + 3) TL();
    }
    HOP(s5, s6);
    Att at;
    float mw0;
    unsigned one = 1;
    KEEP(one);
    while (ctl[C_DONE + wv] == 0) __builtin_amdgcn_s_sleep(1);
    FENCE();
    {
      f32x4 z = *(const f32x4*)(zs + lane * 4);
      float m = fmx(fmx(z[0], z[1]), fmx(z[2], z[3]));
      m = fmx(m, dpp<XOR1>(m)); m = fmx(m, dpp<XOR2>(m)); m = fmx(m, dpp<HMIR>(m));
      mw0 = max_groups(m);
#pragma unroll
      for (int i = 0; i < 4; ++i) { z[i] = __builtin_amdgcn_exp2f((z[i] - mw0) * CEXP); at.acc[i] = f32x2{0.f, 0.f}; }
      *(f32x4*)(zs + lane * 4) = z;
      const float l = sum64((z[0] + z[1]) + (z[2] + z[3]));
      if (lane == 0) *(f32x2*)(lds + L_MRG + 1024 + wv * 2) = f32x2{mw0, l};
      asm volatile("s_mov_b64 exec, 1\n\tds_add_u32 %0, %1\n\ts_mov_b64 exec, -1" :: "v"(lds0 + (L_CTL + C_KEND) * 4), "v"(one) : "memory");
    }
    f32x4 pr;
    const float* const ps = zs + (lane >> 5) * 4;
#define VROW(X, xoff, Y, row) { pr = *(const f32x4*)(ps + (row) * 8); ld4b(X, vr, xoff, voff, fast); LANDED(Y); pv(Y, pr, at); }
#pragma nounroll
    for (int r = 0; r < 28; r += 4) {
      unsigned fast = LEVEL(1);
      if (r == GROW) ctl[C_VMID] = 1;
      HOP(s6, s7);
      VROW(ua.k, ROW(r + 2), ub.k, r)
      HOP(s7, s8);
      VROW(ua.v, ROW(r + 3), ub.v, r + 1) TL();
      fast = LEVEL(0);
      HOP(s8, s9);
      VROW(ub.k, ROW(r + 4), ua.k, r + 2)
      HOP(s9, s10);
      VROW(ub.v, ROW(r + 5), ua.v, r + 3) TL();
    }
    {
      HOP(s10, s11);
      pr = *(const f32x4*)(ps + 28 * 8); ld4<NEND>(ua.k, vr, ROW(30), voff); LANDED(ub.k); pv(ub.k, pr, at);
      HOP(s11, s12);
      pr = *(const f32x4*)(ps + 29 * 8); ld4<NEND>(ua.v, vr, ROW(31), voff); LANDED(ub.v); pv(ub.v, pr, at); TL();
    }
    HOP(s12, s13);
    unsigned cfg;
    while ((cfg = ctl[C_CLR]) == 0) __builtin_amdgcn_s_sleep(1);
    cfg = __builtin_amdgcn_readfirstlane(cfg);
    const float f = __builtin_amdgcn_exp2f((mw0 - __uint_as_float(ctl[C_MAX])) * CEXP) * __uint_as_float(ctl[C_RL]);
#pragma unroll
    for (int i = 0; i < 4; ++i) at.acc[i] *= f;
    pr = *(const f32x4*)(ps + 30 * 8) * f;
    f32x4 p1 = *(const f32x4*)(ps + 31 * 8) * f;
    const int hp = (slot & 15) >> 1, row = (slot & 1) * 16 + xg * 2 + (slot >> 4);
    const unsigned gb = WS_A1 + hp * 81920 + place(cfg, hp);
    unsigned ca = lds0 + (L_CTL + C_MRG) * 4, oa = gb + blk(row * 1024) + voff, om = lane & 12 ? OOB : gb + 65536 + row * 128 + g * 16 + (lane & 3) * 4, tg = ctl[C_TAG];
    unsigned mw = lds0 + (L_MRG + wv * 256 + (lane & 31) * 8 + (lane >> 5) * 4) * 4, mr = lds0 + L_MRG * 4 + voff;
    i32x4 wsd = mksrd(ws, WS_END);
    KEEP(ca); KEEP(oa); KEEP(om); KEEP(tg); KEEP(mw); KEEP(mr); KEEP(pr); KEEP(p1); KEEPS(wsd);
#define QUAD(Y, t, n_) asm volatile("s_waitcnt vmcnt(" #n_ ")" : "+v"(Y[t]), "+v"(at.acc[0]), "+v"(at.acc[1]), "+v"(at.acc[2]), "+v"(at.acc[3]) :: "memory"); pv1(Y[t], pr[t], at);
#define FINAL(h0, h1, h2, h3) { \
      HOP(h0, h1); \
      QUAD(ua.k, 0, 7) QUAD(ua.k, 1, 6) QUAD(ua.k, 2, 5) QUAD(ua.k, 3, 4) pr = p1; \
      HOP(h1, h2); \
      QUAD(ua.v, 0, 3) QUAD(ua.v, 1, 2) QUAD(ua.v, 2, 1) QUAD(ua.v, 3, 0) TL(); \
      STAMP(4); \
      const f32x2 o0 = swap32(at.acc[0][0], at.acc[2][0]), o1 = swap32(at.acc[0][1], at.acc[2][1]), o2 = swap32(at.acc[1][0], at.acc[3][0]), o3 = swap32(at.acc[1][1], at.acc[3][1]); \
      *LDSP(f32x4, mw) = f32x4{o0[0], o1[0], o2[0], o3[0]} + f32x4{o0[1], o1[1], o2[1], o3[1]}; \
      unsigned arr = arrive_issue(ca, one); \
      f32x4 pp[4]; \
      _Pragma("unroll") for (int i = 0; i < 4; ++i) pp[i] = *LDSP(f32x4, mr + i * 1024); \
      asm volatile("" : "+v"(pp[0]), "+v"(pp[1]), "+v"(pp[2]), "+v"(pp[3])); \
      HOP(h2, h3); \
      if (LIKELY(ARRIVED(arr) == 3)) { \
        const u32x4 sv = __builtin_bit_cast(u32x4, (pp[0] + pp[1]) + (pp[2] + pp[3])); \
        unsigned ck = x3(sv[0], sv[1], sv[2]) ^ sv[3];        \
        pub(sv, wsd, oa); \
        ck ^= __float_as_uint(dpp<ROR4>(__uint_as_float(ck))); \
        pub(x3(ck, __float_as_uint(dpp<ROR8>(__uint_as_float(ck))), tg), wsd, om); \
        asm volatile("; published " #h0 ::: "memory"); \
        STAMP(5); \
      } }
    FINAL(s13, s14, s15, ret)
    BARRIER();
  } else {
    const int w = wv - 4;
    const u64 tw0 = __builtin_amdgcn_s_memrealtime();
    unsigned nonce = 0;
    asm volatile("s_cmp_lg_u32 %1, 7\n\ts_cbranch_scc1 1f\n\tglobal_load_dword %0, %0, %2 sc1\n1:" : "+v"(nonce) : "s"(w), "s"(ws) : "memory", "scc");
#define GET_TAG() ({ while (ctl[C_TAGOK] == 0) __builtin_amdgcn_s_sleep(1); (unsigned)__builtin_amdgcn_readfirstlane(ctl[C_TAG]); })
#define TG(v) ((unsigned)((v) >> 32) ^ tagv)
    auto serve = [&]() __attribute__((always_inline)) -> unsigned {
      if (w == 7) { unsigned t_; asm volatile("s_waitcnt vmcnt(0)\n\tv_add_u32 %0, 1, %1" : "=v"(t_) : "v"(nonce) : "memory"); ctl[C_TAG] = t_; ctl[C_TAGOK] = 1; }
      const unsigned tagv = GET_TAG();
      if (w < NPOLL) {
        const unsigned qoff = WS_QG + ((slot * REPQ + xg % REPQ) * 128 + lane) * 8;
        while (ctl[C_QGO] == 0) __builtin_amdgcn_s_sleep(4);
        if (w) __builtin_amdgcn_s_sleep(14);
#pragma nounroll
        for (int it = 0;; ++it) {
          const u64 q0 = ldg(ws, qoff), q1 = ldg(ws, qoff + 512), tm = ldg(ws, WS_T0 + min(lane, 13) * 8);
          if (__all((TG(q0) | TG(q1) | TG(tm)) == 0) || it == SPIN) {
            const unsigned tb = __builtin_amdgcn_readfirstlane((unsigned)tm);
            ctl[C_T0] = (SCH0 + tb + (int)(__uint_as_float(__builtin_amdgcn_readfirstlane(__float_as_uint(sum16(lane < 14 ? (float)(int)((unsigned)tm - tb) : 0.f)))) * (1.f / 14))) << 4;
            ((unsigned*)lds)[L_Q + lane] = (unsigned)q0; ((unsigned*)lds)[L_Q + 64 + lane] = (unsigned)q1;
            LGKM0();
            ctl[C_Q] = 1;
            break;
          }
          if (ctl[C_Q]) break;
        }
      } else {
        if (w == 7) { HOP_CALL(s0); }
        while (ctl[C_Q] == 0) __builtin_amdgcn_s_sleep(4);
      }
      if (w == 1) {
#pragma nounroll
        for (int c = CP0; c < 11; c += CPS) {
          if ((slot >> 1) == 8) {
            unsigned m;
            while (m = ctl[C_MARK + 4 * c + (lane & 3)], !__all(m != 0)) __builtin_amdgcn_s_sleep(8);
            const unsigned m0 = __builtin_amdgcn_readfirstlane(m);
            float d = (float)(int)(m - m0); d += dpp<XOR1>(d); d += dpp<XOR2>(d);
            if (lane == 0) stg(ws, WS_CP + c * 128 + (wg & 15) * 8, m0 + (int)(d * 0.25f) - OFFS(rid), tagv);
          }
          while (ctl[C_MARK + 4 * c + 4] == 0) __builtin_amdgcn_s_sleep(8);
          const u64 tm = ldg(ws, WS_CP + c * 128 + n * 8);
          const unsigned ref = __builtin_amdgcn_readfirstlane(ctl[C_MARK + 4 * c]);
          float late = TG(tm) == 0 ? (float)(int)((unsigned)tm - ref) : 0.f, cntf = TG(tm) == 0 ? 1.f : 0.f;
          late = sum16(late); cntf = sum16(cntf);
          if (cntf >= 8.f) ctl[C_T0] = ((ref + (int)(late / cntf)) << 4) - c * (2 * (SCH1 * 16 / 10));
        }
      } else if (w == 3) {
        while (ctl[C_KEND] < 4) __builtin_amdgcn_s_sleep(32);
        FENCE();
        const f32x2 ml = *(const f32x2*)(lds + L_MRG + 1024 + (lane & 3) * 2);
        float M = fmx(ml[0], dpp<XOR1>(ml[0])); M = fmx(M, dpp<XOR2>(M));
        float L = __builtin_amdgcn_exp2f((ml[0] - M) * CEXP) * ml[1]; L += dpp<XOR1>(L); L += dpp<XOR2>(L);
        if (lane < 2) stg(ws, WS_FL + slot * FLS + xg * 16 + lane * 8, __float_as_uint(lane ? L : M), tagv);
        while (ctl[C_VMID] == 0) __builtin_amdgcn_s_sleep(32);
        u64 cg;
#pragma nounroll
        for (int it = 0; it < SPIN; ++it) {
          cg = ldg(ws, lane < 16 ? WS_FL + slot * FLS + lane * 8 : WS_CFG + min(lane - 16, 9) * CFS);
          if (__all(TG(cg) == 0)) break;
          __builtin_amdgcn_s_sleep(32);
        }
        const float ev = __uint_as_float((unsigned)cg), od = dpp<XOR1>(ev);
        M = lane & 1 ? od : ev;
        const float m0 = M, l0 = lane & 1 ? 0.f : od;
        M = fmx(M, dpp<XOR2>(M)); M = fmx(M, dpp<HMIR>(M)); M = fmx(M, dpp<ROR8>(M));
        L = sum16(__builtin_amdgcn_exp2f((m0 - M) * CEXP) * l0);
        M = __uint_as_float(__builtin_amdgcn_readfirstlane(__float_as_uint(M)));
        const float rl = 1.f / __uint_as_float(__builtin_amdgcn_readfirstlane(__float_as_uint(L)));
        ctl[C_MAX] = __float_as_uint(M); ctl[C_RL] = __float_as_uint(rl);
        LGKM0();
        ctl[C_CLR] = ((unsigned)(__ballot((unsigned)cg & 1u) >> 16) & 0x3ffu) | 0x400u;
      } else if (w >= 4) {
        while (ctl[C_LAND + w - 4] == 0) __builtin_amdgcn_s_sleep(4);
        FENCE();
        const u32x4 q = *(const u32x4*)(lds + L_Q + (lane & 31) * 4);
#pragma nounroll
        for (int r = 0; r < 8; ++r) {
          const u32x4* src = (const u32x4*)(smem + L_BIG + (w - 4) * 32768 + r * 4096) + lane;
          const u32x4 k[4] = {src[0], src[64], src[128], src[192]};
          const float z = qk(k, q, lane & 8);
          if (!(lane & 7)) lds[L_Z + (w - 4) * 256 + r * 8 + zoff] = z;
        }
        LGKM0();
        ctl[C_DONE + w - 4] = 1;
      }
      return tagv;
    };
    auto lead = [&](const int grp, const unsigned base, const int nkb) __attribute__((always_inline)) {
      const unsigned tagv = GET_TAG();
      const unsigned cal = WS_CAL + (grp >> 2) * 16384 + (grp & 3) * 2048;
      __builtin_amdgcn_raw_buffer_store_b128(u32x4{0u, 0u, 0u, 0u}, wsr, lane < 32 && n < NCAL ? (lane >> 4) * 8192 + n * 128 : OOB, cal, SC1);
      WAITVM(0);
      unsigned best0 = ~0u, best1 = ~0u;
#pragma nounroll
      for (int i = 0; i < NCAL; ++i) {
        u64 t0, t1, t2; unsigned j0;
        asm volatile("s_memtime %0\n\ts_waitcnt lgkmcnt(0)\n\ts_load_dword %3, %4, %5\n\ts_waitcnt lgkmcnt(0)\n\ts_memtime %1\n\ts_waitcnt lgkmcnt(0)\n\t"
                     "s_load_dword %3, %4, %6\n\ts_waitcnt lgkmcnt(0)\n\ts_memtime %2\n\ts_waitcnt lgkmcnt(0)"
                     : "=&s"(t0), "=&s"(t1), "=&s"(t2), "=&s"(j0) : "s"(ws), "s"(cal + i * 128), "s"(cal + 8192 + i * 128) : "memory");
        best0 = min(best0, (unsigned)(t1 - t0)); best1 = min(best1, (unsigned)(t2 - t1));
      }
      const unsigned pl = NEAR == 1 ? best1 < best0 : NEAR == 2 ? best1 >= best0 : 0u;
#pragma nounroll
      for (int i = 0; i < nkb; ++i) __builtin_amdgcn_raw_buffer_store_b128(u32x4{SENT, SENT, SENT, SENT}, wsr, voff, base + (pl << 13) + blk(i * 1024), SC1);
      WAITVM(0);
      if (lane == 0) stg(ws, WS_CFG + grp * CFS, pl, tagv);
    };
#define GET_CFG() ({ unsigned c_; while ((c_ = ctl[C_CLR]) == 0) __builtin_amdgcn_s_sleep(SLP); (unsigned)__builtin_amdgcn_readfirstlane(c_); })
    const int qd = lane & 3, q4 = (lane >> 2) & 3;
    i32x4 wsd = mksrd(ws, WS_END);
    if (rid < R_W || rid >= R_NONE) {
      if (rid < R_B && w < 7) {
        {
          u32x4 cv[2]; Tile t;
#pragma unroll
          for (int s = 0; s < 2; ++s) cv[s] = *(const GAS u32x4*)(p.c + (n & 1) * DM + 64 * w + 32 * s + 8 * g);
          tile_load(t, mkrs(p.wm, DM * NMOD * 2), NMOD, (64 * w * NMOD + 64 * rid) * 2, g, n);
          PIN();
          BARRIER();
          u32x4 af[2], bf[2][4];
#pragma unroll
          for (int s = 0; s < 2; ++s)
#pragma unroll
            for (int i = 0; i < 4; ++i) { const float lo = bflo(cv[s][i]), hi = bfhi(cv[s][i]); af[s][i] = pk2(lo * sigm(lo), hi * sigm(hi)); }
          KEEP(af[0]); KEEP(af[1]);
          tile_frags(t, bf);
          f32x4 d[4] = {};
#pragma unroll
          for (int s = 0; s < 2; ++s)
#pragma unroll
            for (int c = 0; c < 4; ++c) d[c] = mfma(af[s], bf[s][c], d[c]);
          if (lane < 16) {
            *(f32x4*)(lds + L_RED + w * 128 + n * 8) = f32x4{d[0][0], d[0][1], d[1][0], d[1][1]};
            *(f32x4*)(lds + L_RED + w * 128 + n * 8 + 4) = f32x4{d[2][0], d[2][1], d[3][0], d[3][1]};
          }
        }
        const unsigned tagv = GET_TAG();
        unsigned arr = arrive_issue(lds0 + (L_CTL + C_A) * 4, 1u);
        if (ARRIVED(arr) == 6) {
          f32x2 s = {0, 0};
#pragma unroll
          for (int i = 0; i < 7; ++i) s += *(const f32x2*)(lds + L_RED + i * 128 + lane * 2);
          const unsigned v = pk2(s[0], s[1]);
#pragma unroll
          for (int i = 0; i < REPM; ++i) if (i == 0 || rid < 14) stg(ws, WS_MODG + (i * NMOD + 64 * rid + lane) * 8, v, tagv);
          if (rid < 14 && lane == 0) stg(ws, WS_T0 + rid * 8, (unsigned)__builtin_amdgcn_s_memrealtime(), tagv);
          if (rid < 14) STAMP(1); else STAMP(9);
        }
      } else if (rid < R_W && w < 7) {
        const int bq = rid - R_B;
        u32x4 xa[2]; unsigned xo[2]; Tile t;
#pragma unroll
        for (int b = 0; b < 2; ++b) {
          xa[b] = __builtin_amdgcn_raw_buffer_load_b128(mkrs(p.x + b * DM, DM * 2), voff, 0, 0);
          xo[b] = ((const GAS u16*)p.x)[b * DM + 64 * w + lane];
        }
        tile_load(t, mkrs(p.wq, DM * HD * 2), HD, (64 * w * HD + 64 * bq) * 2, g, n);
        PIN();
        BARRIER();
        float nrm[2];
#pragma unroll
        for (int b = 0; b < 2; ++b) {
          float s = 0;
#pragma unroll
          for (int i = 0; i < 4; ++i) s += bflo(xa[b][i]) + bfhi(xa[b][i]);
          const float mean = sum64(s) * (1.f / DM);
          float v = 0;
#pragma unroll
          for (int i = 0; i < 4; ++i) { const float lo = bflo(xa[b][i]) - mean, hi = bfhi(xa[b][i]) - mean; v += lo * lo + hi * hi; }
          const float den = rbf(__builtin_sqrtf(rbf(rbf(sum64(lane < 56 ? v : 0.f) * (1.f / DM)) + 1e-5f)));
          nrm[b] = rbf(rbf(bflo(xo[b]) - rbf(mean)) / den);
        }
        KEEP(nrm[0]); KEEP(nrm[1]);
        u32x4 bf[2][4];
        tile_frags(t, bf);
        const unsigned tagv = GET_TAG();
#pragma unroll
        for (int i = 0; i < 8; ++i) KEEP(bf[i >> 2][i & 3]);
        PIN();
        STAMP(9);
        if (BDEL) for (int i = 0; i < 200 && __builtin_amdgcn_s_memrealtime() - tw0 < BDEL; ++i) __builtin_amdgcn_s_sleep(2);
        u64 gs, gc;
#pragma nounroll
        for (int it = 0; it < SPIN; ++it) {
          gs = ldg(ws, WS_MODG + (xg % REPM * NMOD + 64 * w + lane) * 8);
          gc = ldg(ws, WS_MODG + (xg % REPM * NMOD + DM + 64 * w + lane) * 8);
          if (__all((TG(gs) | TG(gc)) == 0)) break;
        }
        STAMP(1);
        const float h0 = rbf(nrm[0] * rbf(1.f + bflo((unsigned)gc))) + bflo((unsigned)gs);
        const float h1 = rbf(nrm[1] * rbf(1.f + bfhi((unsigned)gc))) + bfhi((unsigned)gs);
        const unsigned hp = pk2(h0, h1);
        u16* ht = (u16*)(lds + L_HT + w * 64);
        ht[lane] = (u16)hp; ht[64 + lane] = (u16)(hp >> 16);
        FENCE();
        f32x4 d[4] = {};
#pragma unroll
        for (int s = 0; s < 2; ++s) {
          const u32x4 af = *(const u32x4*)(ht + (n & 1) * 64 + 32 * s + 8 * g);
#pragma unroll
          for (int c = 0; c < 4; ++c) d[c] = mfma(af, bf[s][c], d[c]);
        }
        if (lane < 16) {
          *(f32x4*)(lds + L_RED + w * 128 + n * 4) = f32x4{d[0][0], d[1][0], d[2][0], d[3][0]};
          *(f32x4*)(lds + L_RED + w * 128 + 64 + n * 4) = f32x4{d[0][1], d[1][1], d[2][1], d[3][1]};
        }
        unsigned arr = arrive_issue(lds0 + (L_CTL + C_A) * 4, 1u);
        if (ARRIVED(arr) == 6) {
          f32x2 s = {0, 0};
#pragma unroll
          for (int i = 0; i < 7; ++i) s += *(const f32x2*)(lds + L_RED + i * 128 + lane * 2);
          const unsigned v = pk2(s[0], s[1]);
#pragma unroll
          for (int i = 0; i < REPQ; ++i) stg(ws, WS_QG + ((((lane >> 5) * NH + (bq >> 2)) * REPQ + i) * 128 + (bq & 3) * 32 + (lane & 31)) * 8, v, tagv);
        }
        STAMP(2);
      } else {
        BARRIER();
      }
      serve();
    } else if (rid < R_M) {
      const int hp = (rid - R_W) / 7, wj = (rid - R_W) % 7, head = 2 * hp + (w >> 2);
      const rsrc_t wr = mkrs(p.wo, HD * DM * 2);
      Tile t;
#pragma unroll
      for (int s = 0; s < 2; ++s)
#pragma unroll
        for (int j = 0; j < 8; ++j)
          t.w[s][j] = __builtin_amdgcn_raw_buffer_load_b64(wr, ((16 * (g & 1) + 2 * (g >> 1)) * DM + 4 * n) * 2, ((hp * 512 + 64 * w + 32 * s + 4 * (j >> 1) + (j & 1)) * DM + 64 * wj) * 2, NT);
      PIN();
      BARRIER();
      u32x4 bf[2][4];
      tile_frags(t, bf);
#pragma unroll
      for (int i = 0; i < 8; ++i) KEEP(bf[i >> 2][i & 3]);
      if (w == 2 && wj == 0) lead(hp, 0, 0);
      const unsigned tagv = serve();
      if (w == 2) { HOP_CALL(w0); }
      const unsigned cfg = GET_CFG(), gb = WS_A1 + hp * 81920 + place(cfg, hp), row = (w >> 2) * 16 + (lane >> 2);
      unsigned o1 = gb + blk(row * 1024) + (w & 3) * 256 + qd * 16, o2 = gb + 65536 + row * 128 + (w & 3) * 16 + qd * 4;
      unsigned o3 = WS_A2 + place(cfg, 8) + blk(wj * 4096) + hp * 512 + lane * 8, ca = lds0 + (L_CTL + C_FIN) * 4, one = 1;
      unsigned fw = lds0 + (lane < 32 ? L_FIN + w * 128 + lane * 4 : L_RED + w * 128 + (lane - 32) * 4) * 4, fr = lds0 + (L_FIN + lane * 2) * 4;
      KEEP(o1); KEEP(o2); KEEP(o3); KEEP(ca); KEEP(one); KEEP(fw); KEEP(fr); KEEPS(wsd);
      u32x4 v[4]; unsigned ml;
      asm volatile("" : "=v"(v[0]), "=v"(v[1]), "=v"(v[2]), "=v"(v[3]), "=v"(ml));
      u64 pend = ~0ull; int left = SPIN;
#define W_ROUND() \
        asm volatile("s_mov_b64 exec, %8\n\tbuffer_load_dword %4, %6, %7, 0 offen sc1\n\tbuffer_load_dwordx4 %0, %5, %7, 0 offen sc1\n\t" \
                     "buffer_load_dwordx4 %1, %5, %7, 0 offen offset:64 sc1\n\tbuffer_load_dwordx4 %2, %5, %7, 0 offen offset:128 sc1\n\t" \
                     "buffer_load_dwordx4 %3, %5, %7, 0 offen offset:192 sc1\n\ts_mov_b64 exec, -1\n\ts_waitcnt vmcnt(0)" \
                     : "+v"(v[0]), "+v"(v[1]), "+v"(v[2]), "+v"(v[3]), "+v"(ml) : "v"(o1), "v"(o2), "s"(wsd), "s"(pend) : "memory"); \
        pend = quads(__ballot(x3(x3(x3(v[0][0], v[0][1], v[0][2]), x3(v[0][3], v[1][0], v[1][1]), x3(v[1][2], v[1][3], v[2][0])), \
                            x3(x3(v[2][1], v[2][2], v[2][3]), x3(v[3][0], v[3][1], v[3][2]), v[3][3]), ml) != tagv));
      BARRIER();
      STAMP(3);
      HOP(w0, w1);
#pragma nounroll
      do { const u64 go = GO(left); W_ROUND() pend &= go; } while (pend);
      STAMP(6);
      f32x2 x[8];
#pragma unroll
      for (int i = 0; i < 8; ++i) x[i] = swap16(__uint_as_float(v[2 * (i >> 2)][i & 3]), __uint_as_float(v[2 * (i >> 2) + 1][i & 3]));
      SCHED();
      f32x2 y[4];
#pragma unroll
      for (int i = 0; i < 4; ++i) y[i] = f32x2{x[2 * i][0], x[2 * i + 1][0]} + f32x2{x[2 * i][1], x[2 * i + 1][1]};
      SCHED();
      x[0] = swap32(y[0][0], y[1][0]); x[1] = swap32(y[0][1], y[1][1]); x[2] = swap32(y[2][0], y[3][0]); x[3] = swap32(y[2][1], y[3][1]);
      SCHED();
      y[0] = f32x2{x[0][0], x[1][0]} + f32x2{x[0][1], x[1][1]}; y[1] = f32x2{x[2][0], x[3][0]} + f32x2{x[2][1], x[3][1]};
      SCHED();
      float u[4];
#pragma unroll
      for (int i = 0; i < 4; ++i) { u[i] = y[i >> 1][i & 1] + dpp<ROR8>(y[i >> 1][i & 1]); KEEP(u[i]); }
      const float k0 = __uint_as_float(pk2(u[0], u[1])), k1 = __uint_as_float(pk2(u[2], u[3]));
      HOP(w1, w2);
      const u32x4 af0 = {__float_as_uint(dpp<0x00>(k0)), __float_as_uint(dpp<0x55>(k0)), __float_as_uint(dpp<0xAA>(k0)), __float_as_uint(dpp<0xFF>(k0))};
      const u32x4 af1 = {__float_as_uint(dpp<0x00>(k1)), __float_as_uint(dpp<0x55>(k1)), __float_as_uint(dpp<0xAA>(k1)), __float_as_uint(dpp<0xFF>(k1))};
      f32x4 d[4];
#pragma unroll
      for (int c = 0; c < 4; ++c) d[c] = mfma(af0, bf[0][c], f32x4{0, 0, 0, 0});
#pragma unroll
      for (int c = 0; c < 4; ++c) d[c] = mfma(af1, bf[1][c], d[c]);
      *LDSP(f32x4, fw) = f32x4{d[0][0], d[1][0], d[2][0], d[3][0]};
      unsigned arr = arrive_issue(ca, one);
      f32x2 pp[8];
#pragma unroll
      for (int i = 0; i < 8; ++i) pp[i] = *(const LAS f32x2*)(size_t)(fr + i * 512);
      asm volatile("" : "+v"(pp[0]), "+v"(pp[1]), "+v"(pp[2]), "+v"(pp[3]), "+v"(pp[4]), "+v"(pp[5]), "+v"(pp[6]), "+v"(pp[7]));
      HOP(w2, ret);
      if (LIKELY(ARRIVED(arr) == 7)) {
        const f32x2 s = ((pp[0] + pp[1]) + (pp[2] + pp[3])) + ((pp[4] + pp[5]) + (pp[6] + pp[7]));
        pub(u32x2{nosent(s[0]), nosent(s[1])}, wsd, o3);
      }
      STAMP(7);
    } else if (rid < R_D) {
      const int mj = rid - R_M;
      constexpr int NR = 7, RB = 64;
      u32x4 raw[ME + 1];
      const int wr0 = w ^ (mj & 1) * 4;
      unsigned vo = (lane >> 2) * (FF * 2) + (qd ^ lane >> 5) * 16;
      const unsigned img = L_BIG + wr0 * NR * 1024 + (wr0 >> 2) * 32, so0 = (w & 3) * NR * (16 * FF * 2) + mj * RB;
      {
        const rsrc_t wr = mkrs(wr0 < 4 ? p.wg : p.wu, DM * FF * 2);
#pragma unroll
        for (int i = 0; i < ME; ++i) raw[i] = __builtin_amdgcn_raw_buffer_load_b128(wr, vo, so0 + i * (16 * FF * 2), 0);
      }
      PIN();
      BARRIER();
      if (w == 2 && mj == 0) lead(8, WS_A2, 28);
      const unsigned tagv = serve();
      if (w == 2) { HOP_CALL(m0); }
      while ((ctl[C_DONE] & ctl[C_DONE + 1] & ctl[C_DONE + 2] & ctl[C_DONE + 3]) == 0) __builtin_amdgcn_s_sleep(4);
      FENCE();
#pragma unroll
      for (int i = 0; i < ME; ++i) *(u32x4*)(smem + img + i * 1024 + lane * 16) = raw[i];
      if (lane < 4 && w == 0) lds[L_ST + lane] = 0.f;
      const int b = g & 1, dcol = 64 * min(w, 6) + 4 * n + 2 * (g >> 1);
      const unsigned xw = *(const GAS unsigned*)(p.x + b * DM + dcol);
      u64 mg[6];
#pragma nounroll
      for (int it = 0; it < SPIN; ++it) {
        unsigned bad = 0;
#pragma unroll
        for (int i = 0; i < 6; ++i) { mg[i] = ldg(ws, WS_MODG + ((2 + (i >> 1)) * DM + dcol + (i & 1)) * 8); bad |= TG(mg[i]); }
        if (__all(bad == 0)) break;
      }
      f32x2 ga, sb, cb, xv = {bflo(xw), bfhi(xw)};
#pragma unroll
      for (int i = 0; i < 2; ++i) {
        ga[i] = b ? bfhi((unsigned)mg[i]) : bflo((unsigned)mg[i]); sb[i] = b ? bfhi((unsigned)mg[2 + i]) : bflo((unsigned)mg[2 + i]);
        cb[i] = rbf(1.f + (b ? bfhi((unsigned)mg[4 + i]) : bflo((unsigned)mg[4 + i])));
      }
      KEEP(ga); KEEP(sb); KEEP(cb); KEEP(xv);
      const unsigned cfg = GET_CFG();
      unsigned o1 = w < 7 ? WS_A2 + place(cfg, 8) + blk(w * 4096) + lane * 16 : OOB, o2 = w < 7 && mj == 0 ? WS_X1 + place(cfg, 9) + (b * DM + dcol) * 2 : OOB;
      unsigned o3 = g < 2 && n < 8 && !(n & 1) ? WS_HH + place(cfg, 9) + mj * (2 * RB) + w * 32 + g * 16 + n * 2 : OOB, o4 = w == 3 && mj == 1 && lane == 0 ? WS_NONCE : OOB;
      unsigned sa = lds0 + (L_ST + (w < 7 ? 0 : 4) + b * 2 + (g >> 1)) * 4, sr = lds0 + (L_ST + b * 2) * 4;
      unsigned hw = lds0 + (w < 7 ? L_HT + b * 224 + (dcol >> 1) : L_HT + 448 + lane) * 4, hr = lds0 + (L_HT + (n & 1) * 224 + 4 * g) * 4, tg = tagv;
      KEEP(o1); KEEP(o2); KEEP(o3); KEEP(o4); KEEP(sa); KEEP(sr); KEEP(hw); KEEP(hr); KEEP(tg); KEEPS(wsd);
      u64 rows = 0x0001000100010001ull; float z0 = 0.f, z1 = 0.f;
      KEEPS(rows); KEEP(z0); KEEP(z1);
      u32x4 v[4]; f32x4 s4;
#pragma unroll
      for (int i = 0; i < 4; ++i) { v[i] = u32x4{SENT, SENT, SENT, SENT}; KEEP(v[i]); }
      i32x4 wd2 = mksrd(wr0 < 4 ? p.wg : p.wu, DM * FF * 2);
      unsigned tra = lds0 + L_BIG + (qd >> 1) * (448 * RB + 32) + (8 * g + q4) * RB + ((w & 3) ^ (g & 1)) * 16 + (qd & 1) * 8;
      KEEP(vo); KEEP(tra); KEEPS(wd2);
      HOP(m0, n0);
      BARRIER();
#pragma unroll
      for (int i = ME; i < NR; ++i) dma1(lds0 + img + i * 1024, wd2, so0 + i * (16 * FF * 2), vo);
      if (ME < NR) { WAITVM(0); BARRIER(); }
      u32x4 bf[14];
      if (w < 4) {
#pragma unroll
        for (int s = 0; s < 14; ++s) bf[s] = tr_frag((const unsigned char*)LDSP(unsigned char, tra + 32 * s * RB), 4 * RB);
      }
      PIN();
      STAMP(3);
      HOP(n0, m1);
      {
        int left = SPIN; u64 again;
#pragma nounroll
        do {
          const u64 m0 = __ballot(mx4(v[0]) == SENT), m1 = __ballot(mx4(v[1]) == SENT), m2 = __ballot(mx4(v[2]) == SENT), m3 = __ballot(mx4(v[3]) == SENT);
          const u64 go = GO(left) & (m0 | m1 | m2 | m3);
          asm volatile("s_mov_b64 exec, %6\n\tbuffer_load_dwordx4 %0, %4, %5, 0 offen sc1\n\t"
                       "s_mov_b64 exec, %7\n\tbuffer_load_dwordx4 %1, %4, %5, 0 offen offset:1024 sc1\n\t"
                       "s_mov_b64 exec, %8\n\tbuffer_load_dwordx4 %2, %4, %5, 0 offen offset:2048 sc1\n\t"
                       "s_mov_b64 exec, %9\n\tbuffer_load_dwordx4 %3, %4, %5, 0 offen offset:3072 sc1\n\t"
                       "s_mov_b64 exec, -1\n\ts_waitcnt vmcnt(0)"
                       : "+v"(v[0]), "+v"(v[1]), "+v"(v[2]), "+v"(v[3]) : "v"(o1), "s"(wsd), "s"(m0), "s"(m1), "s"(m2), "s"(m3), "s"(go) : "memory");
          s4 = (__builtin_bit_cast(f32x4, v[0]) + __builtin_bit_cast(f32x4, v[1])) + (__builtin_bit_cast(f32x4, v[2]) + __builtin_bit_cast(f32x4, v[3]));
          again = __ballot(__builtin_isunordered(s4[0] + s4[2], s4[1] + s4[3])) & go;
        } while (again);
      }
      STAMP(6);
      const f32x2 e0 = swap32(s4[0], s4[2]), e1 = swap32(s4[1], s4[3]);
      const f32x2 t = xv + rbf2(ga * rbf2(f32x2{e0[0], e1[0]} + f32x2{e0[1], e1[1]}));
      unsigned x1p = pk2(t[0], t[1]);
      KEEP(x1p);
      const float st = sum16(swap32_add(dot2(x1p, 0x3f803f80u, z0), dot2(x1p, x1p, z1)));
      asm volatile("s_mov_b64 exec, %2\n\tds_add_f32 %0, %1\n\ts_mov_b64 exec, -1" :: "v"(sa), "v"(st), "s"(rows) : "memory");
      BARRIER();
      const f32x2 sq0 = *LDSP(f32x2, sr);
      pub(nosent(x1p), wsd, o2);
      const f32x2 sq = sq0 * (1.f / DM);
      HOP(m1, m2);
      const float rden = __builtin_amdgcn_rcpf(rbf(__builtin_amdgcn_sqrtf(rbf(rbf(fmx(sq[1] - sq[0] * sq[0], 0.f)) + 1e-5f))));
      const f32x2 h = rbf2(rbf2(rbf2(f32x2{bflo(x1p), bfhi(x1p)} - rbf(sq[0])) * rden) * cb) + sb;
      *(LAS unsigned*)(size_t)hw = pk2(h[0], h[1]);
      BARRIER();
      if (LIKELY(w < 4)) {
      u32x4 af[14];
#pragma unroll
      for (int s = 0; s < 14; ++s) af[s] = *LDSP(u32x4, hr + 64 * s);
      PIN();
      f32x4 d = {0, 0, 0, 0};
#pragma unroll
      for (int s = 0; s < 14; ++s) d = mfma(af[s], bf[s], d);
      HOP(m2, ret);
      const float val = rbf(b ? d[1] : d[0]);
      float hh = rbf(sigm(val)) * dpp<ROR8>(val);
      pub(nosent(pk2(hh, dpp<XOR1>(hh))), wsd, o3);
      STAMP(7);
      pub(tg, wsd, o4);
      }
    } else {
      const int dj = rid - R_D, b = g & 1, col = 16 * dj + n;
      u32x4 bf[8]; float gb = 0.f;
      u32x4 raw[DE + 1];
      {
        const rsrc_t wr = mkrs(p.wd, w == 7 ? 0 : FF * DM * 2);
#pragma unroll
        for (int i = 0; i < DE; ++i) raw[i] = __builtin_amdgcn_raw_buffer_load_b128(wr, (lane >> 1) * (DM * 2) + (lane & 1) * 16, (w * 8 + ((i + 2 * dj) & 7)) * (32 * DM * 2) + dj * 32, 0);
      }
      PIN();
      BARRIER();
      if (w == 2 && dj == 0) lead(9, WS_HH, 10);
      const unsigned tagv = serve();
      if (w == 2) { HOP_CALL(d0); }
      if (w < 7) {
        while ((ctl[C_DONE] & ctl[C_DONE + 1]) == 0) __builtin_amdgcn_s_sleep(4);
        FENCE();
#pragma unroll
        for (int i = 0; i < DE; ++i) *(u32x4*)(smem + L_BIG + (w * 8 + ((i + 2 * dj) & 7)) * 1088 + lane * 16) = raw[i];
        FENCE();
      } else {
        u64 gg;
#pragma nounroll
        for (int it = 0; it < SPIN; ++it) { gg = ldg(ws, WS_MODG + (5 * DM + col) * 8); if (__all(TG(gg) == 0)) break; }
        gb = b ? bfhi((unsigned)gg) : bflo((unsigned)gg);
      }
      i32x4 outr = mksrd(p.out, 2 * DM * 2);
      const unsigned cfg = GET_CFG();
      unsigned o1 = w < 7 ? WS_HH + place(cfg, 9) + w * 1024 + lane * 16 : OOB;
      unsigned o2 = lane < 32 ? (b * DM + col) * 2 : OOB, ca = lds0 + (L_CTL + C_FIN) * 4, one = 1;
      unsigned fw = lds0 + ((w < 7 ? L_FIN : L_RED) + w * 64 + (lane & 31) * 2 + (lane >> 5)) * 4, fr = lds0 + (L_FIN + (lane & 31) * 2) * 4;
      u64 k0 = 0xffff0000ffff0000ull, k1 = 0xffffffff00000000ull;
      KEEP(gb); KEEP(o1); KEEP(o2); KEEP(ca); KEEP(one); KEEP(fw); KEEP(fr); KEEPS(wsd); KEEPS(outr); KEEPS(k0); KEEPS(k1);
      i32x4 wd2 = mksrd(p.wd, w == 7 ? 0 : FF * DM * 2);
      unsigned vo = (lane >> 1) * (DM * 2) + (lane & 1) * 16;
      KEEP(vo); KEEPS(wd2);
      HOP(d0, e0);
      BARRIER();
#pragma unroll
      for (int i = DE; i < 8; ++i) dma1(lds0 + L_BIG + (w * 8 + ((i + 2 * dj) & 7)) * 1088, wd2, (w * 8 + ((i + 2 * dj) & 7)) * (32 * DM * 2) + dj * 32, vo);
      WAITVM(0);
#pragma unroll
      for (int s = 0; s < 8; ++s) { bf[s] = tr_frag(smem + L_BIG + (w * 8 + 2 * g + (s >> 2)) * 1088 + (8 * (s & 3) + q4) * 32 + qd * 8, 128); KEEP(bf[s]); }
      STAMP(3);
      if (w == 7) {
        unsigned xw;
#pragma nounroll
        for (int it = 0; it < SPIN; ++it) { FENCE(); xw = __builtin_amdgcn_raw_buffer_load_b32(wsr, WS_X1 + place(cfg, 9) + (b * DM + (col & ~1)) * 2, 0, SC1); if (__all(xw != SENT)) break; }
        if (lane < 32) *(f32x2*)(lds + L_FIN + 448 + lane * 2) = f32x2{__uint_as_float((xw << ((~col & 1) * 16)) & 0xffff0000u), gb};
      }
      u32x4 af;
      asm volatile("" : "=v"(af));
      u64 pend = ~0ull; int left = SPIN;
      HOP(e0, d1);
#pragma nounroll
      do {
        const u64 go = GO(left);
        asm volatile("s_mov_b64 exec, %3\n\tbuffer_load_dwordx4 %0, %1, %2, 0 offen sc1\n\ts_mov_b64 exec, -1\n\ts_waitcnt vmcnt(0)"
                     : "+v"(af) : "v"(o1), "s"(wsd), "s"(pend) : "memory");
        pend = __ballot(mx4(af) == SENT) & go;
      } while (pend);
      STAMP(6);
      f32x4 d[8];
#pragma unroll
      for (int s = 0; s < 8; ++s) d[s] = mfma(af, bf[s], f32x4{0, 0, 0, 0});
      SCHED();
      f32x2 t[4];
#pragma unroll
      for (int i = 0; i < 4; ++i) t[i] = f32x2{d[2 * i][0], d[2 * i][1]} + f32x2{d[2 * i + 1][2], d[2 * i + 1][3]};
      const bool g0 = __builtin_amdgcn_inverse_ballot_w64(k0), g1 = __builtin_amdgcn_inverse_ballot_w64(k1);
      const float l0 = g0 ? t[1][0] : t[0][0], l1 = g0 ? t[1][1] : t[0][1], h0 = g0 ? t[3][0] : t[2][0], h1 = g0 ? t[3][1] : t[2][1];
      const float y = swap16_add(g1 ? h0 : l0, g1 ? h1 : l1);
      asm volatile("" :: "v"(d[0]), "v"(d[1]), "v"(d[2]), "v"(d[3]), "v"(d[4]), "v"(d[5]), "v"(d[6]), "v"(d[7]));
      HOP(d1, ret);
      *(LAS float*)(size_t)fw = y;
      unsigned arr = arrive_issue(ca, one);
      f32x2 pp[8];
#pragma unroll
      for (int i = 0; i < 8; ++i) pp[i] = *(const LAS f32x2*)(size_t)(fr + i * 256);
      asm volatile("" : "+v"(pp[0]), "+v"(pp[1]), "+v"(pp[2]), "+v"(pp[3]), "+v"(pp[4]), "+v"(pp[5]), "+v"(pp[6]), "+v"(pp[7]));
      if (LIKELY(ARRIVED(arr) == 7)) {
        const f32x2 s = ((pp[0] + pp[1]) + (pp[2] + pp[3])) + ((pp[4] + pp[5]) + pp[6]);
        const float r = pp[7][0] + rbf(pp[7][1] * rbf(s[0] + s[1]));
        asm volatile("v_cvt_pk_bf16_f32 %0, %1, %1\n\tbuffer_store_short %0, %2, %3, 0 offen" OUTF : "=&v"(one) : "v"(r), "v"(o2), "s"(outr) : "memory");
      }
      STAMP(7);
    }
  }
}

extern "C" void kernel_launch(void* const* d_in, const int* in_sizes, int n_in, void* d_out, int out_size, void* d_ws, size_t ws_size, hipStream_t stream) {
  Params p;
  p.x = (const u16*)d_in[0]; p.kc = (const u16*)d_in[1]; p.vc = (const u16*)d_in[2]; p.wq = (const u16*)d_in[3]; p.wo = (const u16*)d_in[4];
  p.wg = (const u16*)d_in[6]; p.wu = (const u16*)d_in[7]; p.wd = (const u16*)d_in[8]; p.c = (const u16*)d_in[9]; p.wm = (const u16*)d_in[10];
  p.out = (u16*)d_out; p.ws = (unsigned char*)d_ws;
  unsigned char role[256];
  for (int i = 0; i < 256; ++i) role[i] = R_NONE;
  for (int i = 0; i < 28; ++i) { role[i * 8 + XM] = R_M + i; role[((i + 8) & 31) * 8 + XM + 1] = R_M + 28 + i; role[(i + 4) * 8 + XD] = R_D + i; }
  const int lab[5] = {0, 4, 5, 6, 7};
  for (int hp = 0, pos = 16; hp < 8; ++hp)
    for (int j = 0; j < 7; ++j, ++pos) role[(pos & 31) * 8 + lab[hp < 6 ? hp >> 1 : hp - 3]] = R_W + hp * 7 + j;
  int na = 32;
  for (int s = 0; s < 32; ++s)
    for (int k = 0, rank = 0; k < 8; ++k) {
      unsigned char& r = role[s * 8 + (k < 5 ? lab[(k + s) % 5] : k - 4)];
      if (r != R_NONE) continue;
      if (rank < 2) r = R_B + 2 * s + rank; else if (rank == 2) r = R_A + s; else if (rank == 3 && na < 42 && (s < 6 || s >= 28)) r = R_A + na++;
      ++rank;
    }
  for (int i = 0; i < 64; ++i) p.role[i] = role[4 * i] | role[4 * i + 1] << 8 | role[4 * i + 2] << 16 | (unsigned)role[4 * i + 3] << 24;
  fused_block<<<256, 768, 0, stream>>>(p);
}
```
